```python
import torch

def _softmax(x, axis=-1):
    return torch.softmax(x, dim=axis)
def _gelu(x):
    return torch.nn.functional.gelu(x, approximate="tanh")
def _silu(x):
    return torch.nn.functional.silu(x)
def _sigmoid(x):
    return torch.sigmoid(x)
def _layernorm(x, axis=-1, eps=1e-5):
    m = x.mean(dim=axis, keepdim=True)
    v = x.var(dim=axis, keepdim=True, unbiased=False)
    return (x - m) / torch.sqrt(v + eps)
def _scatter_add(tgt, idx, src, axis=0):
    return tgt.clone().index_add_(axis, idx, src)
def _segment_sum(src, seg, n):
    out = torch.zeros((n,) + src.shape[1:], dtype=src.dtype)
    return out.index_add_(0, seg, src)
def _pad(x, pw):
    flat = tuple(v for pair in reversed(pw) for v in pair)
    return torch.nn.functional.pad(x, flat)

kernel_name = 'parallel_sum__mha_block_cached__glu_gated_glu__decode'

def setup_inputs():

    _w = (lambda a: (lambda t: t.to(torch.bfloat16) if t.is_floating_point() else t)(torch.as_tensor(a)))
    def _A_setup():
        B, Sq, D = 8, 1, 448
        H, Sk, Dh = 16, 4096, 256
        return {
            "x": _w((lambda *s: torch.randn(*s, dtype=torch.bfloat16) * 0.05)(B, Sq, D)),
            "k_cache": _w((lambda *s: torch.randn(*s, dtype=torch.bfloat16) * 3.2)(B, H, Sk, Dh)),
            "v_cache": _w((lambda *s: torch.randn(*s, dtype=torch.bfloat16) * 1.6)(B, H, Sk, Dh)),
            "wq": _w((lambda *s: torch.randn(*s, dtype=torch.bfloat16) * 3.2)(D, H * Dh) * (D ** -0.5)),
            "wo": _w((lambda *s: torch.randn(*s, dtype=torch.bfloat16) * 1.6)(H * Dh, D) * ((H * Dh) ** -0.5)),
        }
    _A_in = _A_setup()

    def _B_setup():
        B, S, D, F = 8, 1, 448, 1792
        return {
            "x": _w((lambda *s: torch.randn(*s, dtype=torch.bfloat16) * 0.05)(B, S, D)),
            "w_gate": _w((lambda *s: torch.randn(*s, dtype=torch.bfloat16) * 0.4)(D, F)),
            "w_up": _w((lambda *s: torch.randn(*s, dtype=torch.bfloat16) * 0.05)(D, F)),
            "w_down": _w((lambda *s: torch.randn(*s, dtype=torch.bfloat16) * 0.05)(F, D)),
        }
    _B_in = _B_setup()

    return {
        "A_x": _A_in["x"],
        "A_k_cache": _A_in["k_cache"],
        "A_v_cache": _A_in["v_cache"],
        "A_wq": _A_in["wq"],
        "A_wo": _A_in["wo"],
        "B_x": _B_in["x"],
        "B_w_gate": _B_in["w_gate"],
        "B_w_up": _B_in["w_up"],
        "B_w_down": _B_in["w_down"],
    }

def reference(A_x, A_k_cache, A_v_cache, A_wq, A_wo, B_x, B_w_gate, B_w_up, B_w_down):
    def _A_ref(x, k_cache, v_cache, wq, wo):
        B, Sq, D = x.shape
        H, Dh = 16, 256
        q = (lambda x, *d: x.permute(*d))((x @ wq).reshape(B, Sq, H, Dh), 0, 2, 1, 3)
        scores = torch.einsum("bhqd,bhkd->bhqk", q, k_cache) * (Dh ** -0.5)
        p = _softmax(scores, axis=-1)
        o = torch.einsum("bhqk,bhkd->bhqd", p, v_cache)
        return (lambda x, *d: x.permute(*d))(o, 0, 2, 1, 3).reshape(B, Sq, H * Dh) @ wo

    def _B_ref(x, w_gate, w_up, w_down):
        g = torch.einsum("bsd,df->bsf", x, w_gate)
        u = torch.einsum("bsd,df->bsf", x, w_up)
        h = _sigmoid(g) * u
        return torch.einsum("bsf,fd->bsd", h, w_down)

    x = A_x
    xn = _layernorm(x, axis=-1)
    return x + _A_ref(xn, A_k_cache, A_v_cache, A_wq, A_wo) + _B_ref(xn, B_w_gate, B_w_up, B_w_down)
```

```cpp
#include <hip/hip_runtime.h>
#include <stdint.h>

typedef unsigned v4u __attribute__((ext_vector_type(4)));
typedef unsigned v2u __attribute__((ext_vector_type(2)));
typedef float v4f __attribute__((ext_vector_type(4)));
typedef float v2f __attribute__((ext_vector_type(2)));
typedef __bf16 v8bf __attribute__((ext_vector_type(8)));
typedef __bf16 v2bf __attribute__((ext_vector_type(2)));
typedef __amdgpu_buffer_rsrc_t rsrc_t;
typedef __attribute__((address_space(3))) volatile v2f lds_v2f;

#define MAP 1
#define NO_GLU 0
#define NO_WO 0
#define NO_WQ 0
#define CH 1
#define QAUX 2
#define WAUX 2

namespace {

constexpr int D_ = 448, DH_ = 256, F_ = 1792;
constexpr unsigned WS_E = 0, WS_REC = 4096, WS_Z = 65536, WS_Y = 1u << 20, WS_SPAN = 2u << 20;
constexpr unsigned SLOT = D_ * 4;
constexpr unsigned OOB = 0x80000000u;
constexpr unsigned SENT = 0xFFFFFFFFu;
constexpr unsigned SPIN_MAX = 1u << 22;
constexpr int AROW = 528;

struct __attribute__((aligned(16))) Lds {
  float xn[D_];
  float qp[8][DH_];
  float p[4][8][2][32];
  float hv[64];
  float ml[4][2];
  float pad[8];
  char a[16 * AROW];
};

__device__ __forceinline__ rsrc_t mk(const void* p, unsigned n) {
  return __builtin_amdgcn_make_buffer_rsrc((void*)p, 0, n, 0x00020000);
}
__device__ __forceinline__ float asf(unsigned u) { return __builtin_bit_cast(float, u); }
__device__ __forceinline__ unsigned asu(float f) { return __builtin_bit_cast(unsigned, f); }
__device__ __forceinline__ v2f unpk(unsigned d) { return v2f{asf(d << 16), asf(d & 0xffff0000u)}; }
template <int C> __device__ __forceinline__ float dpp(float v) {
  return asf(__builtin_amdgcn_update_dpp(0, asu(v), C, 0xf, 0xf, true));
}
constexpr int DPP_X1 = 0xB1, DPP_X2 = 0x4E, DPP_HM = 0x141, DPP_M = 0x140, DPP_R8 = 0x128;
__device__ __forceinline__ float mrg16(float a, float b) {
  auto r = __builtin_amdgcn_permlane16_swap(asu(a), asu(b), false, false);
  return asf(r[0]) + asf(r[1]);
}
__device__ __forceinline__ float mrg32(float a, float b) {
  auto r = __builtin_amdgcn_permlane32_swap(asu(a), asu(b), false, false);
  return asf(r[0]) + asf(r[1]);
}
template <int C> __device__ __forceinline__ float mrg(float a, float b, bool hi) {
  const float keep = hi ? b : a, send = hi ? a : b;
  return keep + dpp<C>(send);
}
__device__ __forceinline__ float wave_sum(float v) {
  v += dpp<DPP_X1>(v); v += dpp<DPP_X2>(v); v += dpp<DPP_HM>(v); v += dpp<DPP_M>(v);
  v = mrg16(v, v); v = mrg32(v, v);
  return v;
}
__device__ __forceinline__ float wave_max(float v) {
  v = fmaxf(v, dpp<DPP_X1>(v)); v = fmaxf(v, dpp<DPP_X2>(v));
  v = fmaxf(v, dpp<DPP_HM>(v)); v = fmaxf(v, dpp<DPP_M>(v));
  auto r = __builtin_amdgcn_permlane16_swap(asu(v), asu(v), false, false);
  v = fmaxf(asf(r[0]), asf(r[1]));
  r = __builtin_amdgcn_permlane32_swap(asu(v), asu(v), false, false);
  return fmaxf(asf(r[0]), asf(r[1]));
}
__device__ __forceinline__ unsigned pkbf(float a, float b) {
  return __builtin_bit_cast(unsigned, __builtin_convertvector(v2f{a, b}, v2bf));
}
__device__ __forceinline__ void wg_barrier() { asm volatile("s_waitcnt lgkmcnt(0)\n\ts_barrier" ::: "memory"); }
__device__ __forceinline__ float ex2(float x) { return __builtin_amdgcn_exp2f(x); }

#define STAMP(k) do { } while (0)

}

__global__ void __launch_bounds__(768, 3)
fused_decode_kernel(const char* __restrict__ x, const char* __restrict__ kc, const char* __restrict__ vc,
                    const char* __restrict__ wq, const char* __restrict__ wo, const char* __restrict__ wg,
                    const char* __restrict__ wu, const char* __restrict__ wd, char* out, char* ws) {
  __shared__ Lds L;
  const int lane = threadIdx.x & 63;
  const int w = __builtin_amdgcn_readfirstlane(threadIdx.x >> 6);
  const int bx = blockIdx.x, b = bx >> 5, h = ((bx >> 1) & 3) * 4 + ((bx >> 3) & 3), j = h * 2 + (bx & 1), bid = b * 32 + j;
  const int gs = (bx & 7) * 4 + ((bx >> 3) & 3);
  const int half = lane >> 5, l31 = lane & 31;

  if (w < 4) {
    if (w == 0) STAMP(0);
    const char* kb = kc + (size_t)bid * (1u << 20);
    const char* vb = vc + (size_t)bid * (1u << 20);
    const int voff = w * (CH * 1024) + lane * 16;
    auto so = [](int i) -> unsigned { return (unsigned)((i / CH) * 4 * CH + i % CH) * 1024u; };
    v4u a[32];
    {
      const rsrc_t rk = mk(kb, 1u << 20);
#pragma unroll
      for (int i = 0; i < 32; ++i) a[i] = __builtin_amdgcn_raw_buffer_load_b128(rk, voff, so(i), 2);
    }
    __builtin_amdgcn_sched_barrier(0);
    wg_barrier();
    v2f q[4];
    {
      v4f s0 = {0, 0, 0, 0}, s1 = {0, 0, 0, 0};
#pragma unroll
      for (int k = 0; k < 4; ++k) {
        const v4f* p = (const v4f*)&L.qp[half * 4 + k][l31 * 8];
        s0 += p[0]; s1 += p[1];
      }
      const float c = 0.0625f * 1.44269504088896340736f;
      q[0] = v2f{mrg32(s0[0], s0[0]) * c, mrg32(s0[1], s0[1]) * c};
      q[1] = v2f{mrg32(s0[2], s0[2]) * c, mrg32(s0[3], s0[3]) * c};
      q[2] = v2f{mrg32(s1[0], s1[0]) * c, mrg32(s1[1], s1[1]) * c};
      q[3] = v2f{mrg32(s1[2], s1[2]) * c, mrg32(s1[3], s1[3]) * c};
    }
    if (w == 0) STAMP(1);
    const bool b3 = lane & 8, b2 = lane & 4, b1 = lane & 2, b0 = lane & 1;
    const int rev = __builtin_bitreverse32(l31) >> 27;
    float* pw = &L.p[w][0][half][rev];
    float mx = -__builtin_inff();
#pragma unroll 1
    for (int r = 0; r < 8; ++r) {
      const rsrc_t rn = mk(r < 7 ? kb : vb, 1u << 20);
      const unsigned sb = (unsigned)((r + 1) & 7) << 17;
      float p0 = 0, p1 = 0, p2 = 0, p3 = 0, p4 = 0;
#pragma unroll
      for (int i = 0; i < 32; ++i) {
        v2f s = unpk(a[i][0]) * q[0];
        s = __builtin_elementwise_fma(unpk(a[i][1]), q[1], s);
        s = __builtin_elementwise_fma(unpk(a[i][2]), q[2], s);
        s = __builtin_elementwise_fma(unpk(a[i][3]), q[3], s);
        float v = s[0] + s[1];
        asm volatile("" : "+v"(v));
        a[i] = __builtin_amdgcn_raw_buffer_load_b128(rn, voff, sb + so(i), 2);
        if (i & 1) { v = mrg16(p0, v);
          if (i & 2) { v = mrg<DPP_M>(p1, v, b3);
            if (i & 4) { v = mrg<DPP_HM>(p2, v, b2);
              if (i & 8) { v = mrg<DPP_X2>(p3, v, b1);
                if (i & 16) { v = mrg<DPP_X1>(p4, v, b0); pw[r * 64] = v; mx = fmaxf(mx, v); }
                else p4 = v; }
              else p3 = v; }
            else p2 = v; }
          else p1 = v; }
        else p0 = v;
        __builtin_amdgcn_sched_barrier(0);
      }
    }
    if (w == 0) STAMP(2);
    {
      mx = wave_max(mx);
      float sum = 0;
#pragma unroll
      for (int r = 0; r < 8; ++r) { const float p = ex2(pw[r * 64] - mx); pw[r * 64] = p; sum += p; }
      sum = wave_sum(sum);
      if (lane == 0) *(lds_v2f*)&L.ml[w][0] = v2f{mx, sum};
    }
    v2f acc[4] = {{0, 0}, {0, 0}, {0, 0}, {0, 0}};
    const v4f* pr = (const v4f*)&L.p[w][0][half][0];
#pragma unroll 1
    for (int r = 0; r < 8; ++r) {
      const rsrc_t rn = mk(vb, r < 7 ? 1u << 20 : 0u);
      const unsigned sb = (unsigned)((r + 1) & 7) << 17;
      v4f pv = pr[r * 16];
#pragma unroll
      for (int i = 0; i < 32; ++i) {
        const float p = pv[i & 3];
        if ((i & 3) == 3 && i < 31) pv = pr[r * 16 + (i + 1) / 4];
        const v2f pp = {p, p};
#pragma unroll
        for (int e = 0; e < 4; ++e) acc[e] = __builtin_elementwise_fma(unpk(a[i][e]), pp, acc[e]);
        asm volatile("" : "+v"(acc[0]), "+v"(acc[1]), "+v"(acc[2]), "+v"(acc[3]));
        a[i] = __builtin_amdgcn_raw_buffer_load_b128(rn, voff, sb + so(i), 2);
        __builtin_amdgcn_sched_barrier(0);
      }
    }
    {
      v4u hi, lo;
#pragma unroll
      for (int e = 0; e < 4; ++e) {
        hi[e] = pkbf(acc[e][0], acc[e][1]);
        const v2f r = acc[e] - unpk(hi[e]);
        lo[e] = pkbf(r[0], r[1]);
      }
      char* ap = L.a + (w * 4 + half * 2) * AROW + l31 * 16;
      *(v4u*)ap = hi;
      *(v4u*)(ap + AROW) = lo;
    }
    if (w == 0) STAMP(3);
    wg_barrier();
    return;
  }

  const int hw = w - 4;
  const rsrc_t rws = mk(ws, WS_SPAN);
  v2u ep = {0, 0};
  {
    const v4u xr = __builtin_amdgcn_raw_buffer_load_b128(mk(x + b * (D_ * 2), D_ * 2), lane * 16, 0, QAUX);
    const rsrc_t rq = mk(wq + h * (DH_ * 2), NO_WQ ? 0 : D_ * 8192 - h * (DH_ * 2));
    const int row0 = hw * 56 + half * 28;
    v4u t[28];
#pragma unroll
    for (int i = 0; i < 28; ++i) t[i] = __builtin_amdgcn_raw_buffer_load_b128(rq, row0 * 8192 + l31 * 16, i * 8192, QAUX);
    if (hw == 7) {
      ep = __builtin_amdgcn_raw_buffer_load_b64(rws, WS_E + b * 256, 0, 16);
      if (lane < 4) *(lds_v2f*)&L.ml[lane][0] = v2f{0.f, -1.f};
    }
    __builtin_amdgcn_sched_barrier(0);
    v2f xv[4];
#pragma unroll
    for (int e = 0; e < 4; ++e) xv[e] = unpk(xr[e]);
    const v2f s2 = (xv[0] + xv[1]) + (xv[2] + xv[3]);
    const float mean = wave_sum(s2[0] + s2[1]) * (1.0f / D_);
    float ss = 0;
#pragma unroll
    for (int e = 0; e < 4; ++e) { xv[e] -= mean; ss += xv[e][0] * xv[e][0] + xv[e][1] * xv[e][1]; }
    ss = lane < 56 ? ss : 0.f;
    const float rstd = 1.0f / sqrtf(wave_sum(ss) * (1.0f / D_) + 1e-5f);
    if (lane < 56) {
      v4f* p = (v4f*)&L.xn[lane * 8];
      p[0] = v4f{xv[0][0] * rstd, xv[0][1] * rstd, xv[1][0] * rstd, xv[1][1] * rstd};
      p[1] = v4f{xv[2][0] * rstd, xv[2][1] * rstd, xv[3][0] * rstd, xv[3][1] * rstd};
    }
    const v4f* xp = (const v4f*)&L.xn[row0];
    v2f acc[4] = {{0, 0}, {0, 0}, {0, 0}, {0, 0}};
#pragma unroll
    for (int g = 0; g < 7; ++g) {
      const v4f xx = xp[g];
#pragma unroll
      for (int k = 0; k < 4; ++k) {
        const v2f xs = {xx[k], xx[k]};
#pragma unroll
        for (int e = 0; e < 4; ++e) acc[e] = __builtin_elementwise_fma(unpk(t[g * 4 + k][e]), xs, acc[e]);
      }
    }
    const v4f o = {mrg32(acc[0][0], acc[2][0]), mrg32(acc[0][1], acc[2][1]), mrg32(acc[1][0], acc[3][0]), mrg32(acc[1][1], acc[3][1])};
    *(v4f*)&L.qp[hw][l31 * 8 + half * 4] = o;
  }
  wg_barrier();

  if (hw < 7) {
    const int kg = lane >> 4, nn = lane & 15;
    const rsrc_t ro = mk(wo + (size_t)h * (DH_ * D_ * 2), NO_WO ? 0 : DH_ * D_ * 2);
    const int vo = kg * 8 * (D_ * 2) + hw * 128 + nn * 8;
    v4u bf[8][4];
#pragma unroll
    for (int kp = 0; kp < 4; ++kp) {
      v2u t[2][8];
#pragma unroll
      for (int u = 0; u < 2; ++u)
#pragma unroll
        for (int e = 0; e < 8; ++e) t[u][e] = __builtin_amdgcn_raw_buffer_load_b64(ro, vo, ((kp * 2 + u) * 32 + e) * (D_ * 2), WAUX);
#pragma unroll
      for (int u = 0; u < 2; ++u)
#pragma unroll
        for (int d = 0; d < 4; ++d) {
          bf[kp * 2 + u][0][d] = __builtin_amdgcn_perm(t[u][2 * d + 1][0], t[u][2 * d][0], 0x05040100u);
          bf[kp * 2 + u][1][d] = __builtin_amdgcn_perm(t[u][2 * d + 1][0], t[u][2 * d][0], 0x07060302u);
          bf[kp * 2 + u][2][d] = __builtin_amdgcn_perm(t[u][2 * d + 1][1], t[u][2 * d][1], 0x05040100u);
          bf[kp * 2 + u][3][d] = __builtin_amdgcn_perm(t[u][2 * d + 1][1], t[u][2 * d][1], 0x07060302u);
        }
#pragma unroll
      for (int u = 0; u < 2; ++u)
        asm volatile("" : "+v"(bf[kp * 2 + u][0]), "+v"(bf[kp * 2 + u][1]), "+v"(bf[kp * 2 + u][2]), "+v"(bf[kp * 2 + u][3]));
      __builtin_amdgcn_sched_barrier(0);
    }
    wg_barrier();
    const char* ap = L.a + nn * AROW + kg * 16;
    v4f dd[4] = {{0, 0, 0, 0}, {0, 0, 0, 0}, {0, 0, 0, 0}, {0, 0, 0, 0}};
    const v4f m01 = *(const v4f*)&L.ml[0][0], m23 = *(const v4f*)&L.ml[2][0];
    const float mk_ = L.ml[kg][0];
#pragma unroll
    for (int ks = 0; ks < 8; ++ks) {
      const v8bf af = *(const v8bf*)(ap + ks * 64);
#pragma unroll
      for (int t = 0; t < 4; ++t)
        dd[t] = __builtin_amdgcn_mfma_f32_16x16x32_bf16(af, __builtin_bit_cast(v8bf, bf[ks][t]), dd[t], 0, 0, 0);
    }
    const float sc = ex2(mk_ - fmaxf(fmaxf(m01[0], m01[2]), fmaxf(m23[0], m23[2])));
    float y[4];
#pragma unroll
    for (int t = 0; t < 4; ++t) y[t] = ((dd[t][0] + dd[t][1]) + (dd[t][2] + dd[t][3])) * sc;
    const float yy = mrg32(mrg16(y[0], y[1]), mrg16(y[2], y[3]));
    const unsigned yb = min(asu(yy), SENT - 1u);
    __builtin_amdgcn_raw_buffer_store_b32(yb, rws, WS_Y + bid * SLOT + hw * 256 + nn * 16 + kg * 4, 0, 16);
    if (hw == 0) STAMP(4);
    return;
  }

  const unsigned lo56 = lane < 56 ? (unsigned)lane * 32u : OOB;
  {
    const v4u s = {SENT, SENT, SENT, SENT};
    __builtin_amdgcn_raw_buffer_store_b128(s, rws, WS_Y + bid * SLOT + lo56, 0, 16);
    __builtin_amdgcn_raw_buffer_store_b128(s, rws, WS_Y + bid * SLOT + 16 + lo56, 0, 16);
  }
  if (gs < 28) {
    const int rg = lane >> 3;
    const rsrc_t rgt = mk(wg, NO_GLU ? 0 : D_ * F_ * 2), rup = mk(wu, NO_GLU ? 0 : D_ * F_ * 2);
    const int vo = rg * 56 * (F_ * 2) + gs * 128 + (lane & 7) * 16;
    v2f g[4] = {{0, 0}, {0, 0}, {0, 0}, {0, 0}}, u[4] = {{0, 0}, {0, 0}, {0, 0}, {0, 0}};
#pragma unroll 1
    for (int c = 0; c < 7; ++c) {
      v4u tg[8], tu[8];
#pragma unroll
      for (int i = 0; i < 8; ++i) {
        tg[i] = __builtin_amdgcn_raw_buffer_load_b128(rgt, vo, (c * 8 + i) * (F_ * 2), WAUX);
        tu[i] = __builtin_amdgcn_raw_buffer_load_b128(rup, vo, (c * 8 + i) * (F_ * 2), WAUX);
      }
      const v4f x0 = *(const v4f*)&L.xn[rg * 56 + c * 8], x1 = *(const v4f*)&L.xn[rg * 56 + c * 8 + 4];
#pragma unroll
      for (int i = 0; i < 8; ++i) {
        const float xs = i < 4 ? x0[i & 3] : x1[i & 3];
        const v2f xx = {xs, xs};
#pragma unroll
        for (int e = 0; e < 4; ++e) {
          g[e] = __builtin_elementwise_fma(unpk(tg[i][e]), xx, g[e]);
          u[e] = __builtin_elementwise_fma(unpk(tu[i][e]), xx, u[e]);
        }
      }
    }
    float hv[8];
#pragma unroll
    for (int e = 0; e < 8; ++e) {
      float gg = g[e >> 1][e & 1], uu = u[e >> 1][e & 1];
      gg += dpp<DPP_R8>(gg); gg = mrg16(gg, gg); gg = mrg32(gg, gg);
      uu += dpp<DPP_R8>(uu); uu = mrg16(uu, uu); uu = mrg32(uu, uu);
      hv[e] = uu / (1.0f + expf(-gg));
    }
    if (lane < 8) {
      *(v4f*)&L.hv[lane * 8] = v4f{hv[0], hv[1], hv[2], hv[3]};
      *(v4f*)&L.hv[lane * 8 + 4] = v4f{hv[4], hv[5], hv[6], hv[7]};
    }
    const rsrc_t rd = mk(wd + gs * (64 * D_ * 2), NO_GLU ? 0 : 64 * D_ * 2);
    const unsigned vd = lane < 56 ? (unsigned)lane * 16u : OOB;
    v2f z[4] = {{0, 0}, {0, 0}, {0, 0}, {0, 0}};
#pragma unroll 1
    for (int c = 0; c < 4; ++c) {
      v4u t[16];
#pragma unroll
      for (int i = 0; i < 16; ++i) t[i] = __builtin_amdgcn_raw_buffer_load_b128(rd, vd, (c * 16 + i) * (D_ * 2), WAUX);
#pragma unroll
      for (int i4 = 0; i4 < 4; ++i4) {
        const v4f hh = *(const v4f*)&L.hv[c * 16 + i4 * 4];
#pragma unroll
        for (int k = 0; k < 4; ++k) {
          const v2f hs = {hh[k], hh[k]};
#pragma unroll
          for (int e = 0; e < 4; ++e) z[e] = __builtin_elementwise_fma(unpk(t[i4 * 4 + k][e]), hs, z[e]);
        }
      }
    }
    __builtin_amdgcn_raw_buffer_store_b128(v4u{asu(z[0][0]), asu(z[0][1]), asu(z[1][0]), asu(z[1][1])}, rws, WS_Z + (b * 32 + gs) * SLOT + lo56, 0, 16);
    __builtin_amdgcn_raw_buffer_store_b128(v4u{asu(z[2][0]), asu(z[2][1]), asu(z[3][0]), asu(z[3][1])}, rws, WS_Z + (b * 32 + gs) * SLOT + 16 + lo56, 0, 16);
  }
  STAMP(5);
  const unsigned long long tag = (((unsigned long long)ep[1] << 32) | ep[0]) + 1ull;
  const unsigned tlo = (unsigned)tag, thi = (unsigned)(tag >> 32);
  {
    v2f ml;
#pragma unroll 1
    for (unsigned spin = 0; spin < SPIN_MAX; ++spin) {
      ml = *(lds_v2f*)&L.ml[lane & 3][0];
      if (__builtin_amdgcn_ballot_w64(asu(ml[1]) == 0xBF800000u) == 0) break;
      __builtin_amdgcn_s_sleep(16);
    }
    float m = fmaxf(ml[0], dpp<DPP_X1>(ml[0]));
    m = fmaxf(m, dpp<DPP_X2>(m));
    float l = ml[1] * ex2(ml[0] - m);
    l += dpp<DPP_X1>(l);
    l += dpp<DPP_X2>(l);
    asm volatile("s_waitcnt vmcnt(0)" ::: "memory");
    const v2u gr = (lane & 1) ? v2u{asu(l), thi} : v2u{asu(m), tlo};
    __builtin_amdgcn_raw_buffer_store_b64(gr, rws, lane < 2 ? WS_REC + bid * 16 + lane * 8 : OOB, 0, 16);
  }
  if (j >= 28) { wg_barrier(); return; }
  float wgt;
  {
    v2u g0, g1;
#pragma unroll 1
    for (unsigned spin = 0; spin < SPIN_MAX; ++spin) {
      g0 = __builtin_amdgcn_raw_buffer_load_b64(rws, WS_REC + (b * 32 + l31) * 16, 0, 16);
      g1 = __builtin_amdgcn_raw_buffer_load_b64(rws, WS_REC + (b * 32 + l31) * 16 + 8, 0, 16);
      if (__builtin_amdgcn_ballot_w64(g0[1] != tlo || g1[1] != thi) == 0) break;
      __builtin_amdgcn_s_sleep(64);
    }
    asm volatile("" ::: "memory");
    if (j == 0) __builtin_amdgcn_raw_buffer_store_b64(v2u{tlo, thi}, rws, lane == 0 ? WS_E + b * 256 : OOB, 0, 16);
    const float m = asf(g0[0]), l = asf(g1[0]);
    const float M = fmaxf(m, dpp<DPP_X1>(m));
    const float e = ex2(m - M);
    const float le = l * e;
    wgt = e / (le + dpp<DPP_X1>(le));
  }
  const unsigned vy = (b * 32 + l31) * SLOT + j * 64 + half * 32;
  const unsigned vz = l31 < 28 ? WS_Z + vy : OOB;
  const v4u z0 = __builtin_amdgcn_raw_buffer_load_b128(rws, vz, 0, 16);
  const v4u z1 = __builtin_amdgcn_raw_buffer_load_b128(rws, vz + 16, 0, 16);
  const int col = j * 16 + half * 8 + ((lane >> 4) & 1) + ((lane >> 2) & 2) + (lane & 4);
  const float xres = asf((unsigned)__builtin_amdgcn_raw_buffer_load_b16(mk(x + b * (D_ * 2), D_ * 2), col * 2, 0, 0) << 16);
  STAMP(6);
  wg_barrier();
  v4u y0, y1;
#pragma unroll 1
  for (unsigned spin = 0; spin < SPIN_MAX; ++spin) {
    y0 = __builtin_amdgcn_raw_buffer_load_b128(rws, WS_Y + vy, 0, 16);
    y1 = __builtin_amdgcn_raw_buffer_load_b128(rws, WS_Y + vy + 16, 0, 16);
    const unsigned mx4 = max(max(max(y0[0], y0[1]), max(y0[2], y0[3])), max(max(y1[0], y1[1]), max(y1[2], y1[3])));
    if (__builtin_amdgcn_ballot_w64(mx4 == SENT) == 0) break;
    __builtin_amdgcn_s_sleep(2);
  }
  float v[8];
#pragma unroll
  for (int e = 0; e < 4; ++e) { v[e] = fmaf(wgt, asf(y0[e]), asf(z0[e])); v[e + 4] = fmaf(wgt, asf(y1[e]), asf(z1[e])); }
  const bool b3 = lane & 8, b2 = lane & 4;
  float r = mrg<DPP_HM>(mrg<DPP_M>(mrg16(v[0], v[1]), mrg16(v[2], v[3]), b3), mrg<DPP_M>(mrg16(v[4], v[5]), mrg16(v[6], v[7]), b3), b2);
  r += dpp<DPP_X2>(r);
  r += dpp<DPP_X1>(r);
  const unsigned ob = pkbf(r + xres, 0.f);
  __builtin_amdgcn_raw_buffer_store_b16((unsigned short)ob, mk(out + b * (D_ * 2), D_ * 2), (lane & 3) == 0 ? (unsigned)col * 2u : OOB, 0, 0);
  STAMP(7);
}

extern "C" void kernel_launch(void* const* d_in, const int* in_sizes, int n_in, void* d_out, int out_size,
                              void* d_ws, size_t ws_size, hipStream_t stream) {
  (void)in_sizes; (void)n_in; (void)out_size; (void)ws_size;
  fused_decode_kernel<<<256, 768, 0, stream>>>((const char*)d_in[0], (const char*)d_in[1], (const char*)d_in[2],
                                               (const char*)d_in[3], (const char*)d_in[4], (const char*)d_in[6],
                                               (const char*)d_in[7], (const char*)d_in[8], (char*)d_out, (char*)d_ws);
}
```

```cpp
#include <hip/hip_runtime.h>
#include <stdint.h>

typedef unsigned v4u __attribute__((ext_vector_type(4)));
typedef unsigned v2u __attribute__((ext_vector_type(2)));
typedef float v4f __attribute__((ext_vector_type(4)));
typedef float v2f __attribute__((ext_vector_type(2)));
typedef __bf16 v8bf __attribute__((ext_vector_type(8)));
typedef __bf16 v2bf __attribute__((ext_vector_type(2)));
typedef __amdgpu_buffer_rsrc_t rsrc_t;
typedef __attribute__((address_space(3))) volatile v2f lds_v2f;
typedef __attribute__((address_space(3))) volatile unsigned lds_u;
typedef short v4s __attribute__((ext_vector_type(4)));
typedef short v8s __attribute__((ext_vector_type(8)));

#define SH 0
#define ROLE 2
#define QG 0
#define QS 0
#define NOQ 0
#define QW 1
#define XAUX 0
#define ABL 0
#define GOV 0
#define PN 107
#define DELTA 20
#define GPD 250
#define EARLY 0
#define QAUX 2
#define PF 8
#define WD 8
#define SPRIO 3
#define PACE 0
#define PACE0 150
#define WO0 800
#define WOD 700
#define WOS 100
#define GL0 750
#define GLD 110
#define MAP 5
#define NO_GLU 0
#define NO_WO 0
#define WAUX 2

namespace {

constexpr int D_ = 448, DH_ = 256, F_ = 1792;
constexpr unsigned WS_E = 0, WS_P = 4096, WS_REC = 32768, WS_Q = 65536, WS_Y = 256u << 10, WS_SPAN = 2u << 20;
constexpr unsigned SLOT = D_ * 4;
constexpr unsigned OOB = 0x80000000u;
constexpr unsigned SPIN_MAX = 1u << 22;
constexpr int AROW = 528;
constexpr int XROW = 912;

struct __attribute__((aligned(16))) Lds {
  float xn[D_];
  char xa[8 * XROW];
  char wqs[D_ * 32];
  float z[D_];
  unsigned q[DH_ / 2];
  unsigned tag[2];
  unsigned gate;
  float wgt;
  unsigned lag[4];
  float p[4][8][2][32];
  float hv[64];
  float ml[12][2];
  char a[16 * AROW];
};

__device__ __forceinline__ rsrc_t mk(const void* p, unsigned n) {
  return __builtin_amdgcn_make_buffer_rsrc((void*)p, 0, n, 0x00020000);
}
__device__ __forceinline__ float asf(unsigned u) { return __builtin_bit_cast(float, u); }
__device__ __forceinline__ unsigned asu(float f) { return __builtin_bit_cast(unsigned, f); }
__device__ __forceinline__ v2f unpk(unsigned d) { return v2f{asf(d << 16), asf(d & 0xffff0000u)}; }
template <int C> __device__ __forceinline__ float dpp(float v) {
  return asf(__builtin_amdgcn_update_dpp(0, asu(v), C, 0xf, 0xf, true));
}
constexpr int DPP_X1 = 0xB1, DPP_X2 = 0x4E, DPP_HM = 0x141, DPP_M = 0x140, DPP_R8 = 0x128;
__device__ __forceinline__ float mrg16(float a, float b) {
  auto r = __builtin_amdgcn_permlane16_swap(asu(a), asu(b), false, false);
  return asf(r[0]) + asf(r[1]);
}
__device__ __forceinline__ float mrg32(float a, float b) {
  auto r = __builtin_amdgcn_permlane32_swap(asu(a), asu(b), false, false);
  return asf(r[0]) + asf(r[1]);
}
template <int C> __device__ __forceinline__ float mrg(float a, float b, bool hi) {
  const float keep = hi ? b : a, send = hi ? a : b;
  return keep + dpp<C>(send);
}
__device__ __forceinline__ float wave_sum(float v) {
  v += dpp<DPP_X1>(v); v += dpp<DPP_X2>(v); v += dpp<DPP_HM>(v); v += dpp<DPP_M>(v);
  v = mrg16(v, v); v = mrg32(v, v);
  return v;
}
__device__ __forceinline__ float wave_max(float v) {
  v = fmaxf(v, dpp<DPP_X1>(v)); v = fmaxf(v, dpp<DPP_X2>(v));
  v = fmaxf(v, dpp<DPP_HM>(v)); v = fmaxf(v, dpp<DPP_M>(v));
  auto r = __builtin_amdgcn_permlane16_swap(asu(v), asu(v), false, false);
  v = fmaxf(asf(r[0]), asf(r[1]));
  r = __builtin_amdgcn_permlane32_swap(asu(v), asu(v), false, false);
  return fmaxf(asf(r[0]), asf(r[1]));
}
__device__ __forceinline__ unsigned pkbf(float a, float b) {
  return __builtin_bit_cast(unsigned, __builtin_convertvector(v2f{a, b}, v2bf));
}
__device__ __forceinline__ float rbf(float a) { return asf(pkbf(a, 0.f) << 16); }
__device__ __forceinline__ float dot2(unsigned a, unsigned b, float c) {
  return __builtin_amdgcn_fdot2_f32_bf16(__builtin_bit_cast(v2bf, a), __builtin_bit_cast(v2bf, b), c, false);
}
__device__ __forceinline__ void wg_barrier() { asm volatile("s_waitcnt lgkmcnt(0)\n\ts_barrier" ::: "memory"); }
__device__ __forceinline__ float ex2(float x) { return __builtin_amdgcn_exp2f(x); }

#define TNOW(v) do { } while (0)
#define STAMP(k) do { } while (0)
#define STAMPV(k, v) do { } while (0)

}

__global__ void __launch_bounds__(768, 3)
fused_decode_kernel(const char* __restrict__ x, const char* __restrict__ kc, const char* __restrict__ wq, char* ws,
                    const char* __restrict__ vc, const char* __restrict__ wo, const char* __restrict__ wg,
                    const char* __restrict__ wu, const char* __restrict__ wd, char* out) {
  __shared__ Lds L;
  asm volatile("" ::"s"(x), "s"(kc), "s"(wq), "s"(ws));
  const int lane = threadIdx.x & 63;
  const int wp = __builtin_amdgcn_readfirstlane(threadIdx.x >> 6);
  const int w = ROLE == 0 ? wp : ROLE == 1 ? (wp < 8 ? wp + 4 : wp - 8) : 11 - wp;
  const int bx = blockIdx.x, b = bx >> 5, gs = bx & 31;
  const int j = (bx & 7) * 2 + ((bx >> 3) & 1) + (bx & 16);
  const int h = j >> 1, bid = b * 32 + j;
  const int half = lane >> 5, l31 = lane & 31;
  const int hw = w - 4;
  const unsigned t0 = (unsigned)__builtin_amdgcn_s_memrealtime();
  const char* kb = kc + (size_t)bid * (1u << 20);
  const int voff = w * 1024 + lane * 16;
  const int qg = j * 8 + b;
  const rsrc_t rws = mk(ws, WS_SPAN);
  v4u a[32];
  v2u ep;
  {
    if (w < 4) {
      const rsrc_t rk = mk(kb, 1u << 20);
#pragma unroll
      for (int i = 0; i < PF; ++i) a[i] = __builtin_amdgcn_raw_buffer_load_b128(rk, voff, i * 4096, 2);
    } else {
      const rsrc_t r1 = mk(wq + qg * 32, 447 * 8192 + 32);
      const int vq = (lane >> 1) * 8192 + (lane & 1) * 16;
      a[0] = __builtin_amdgcn_raw_buffer_load_b128(mk(x + hw * (D_ * 2), D_ * 2), lane * 16, 0, XAUX);
      a[1] = __builtin_amdgcn_raw_buffer_load_b128(r1, vq, hw * (56 * 8192), QAUX);
      a[2] = __builtin_amdgcn_raw_buffer_load_b128(r1, lane < 48 ? vq : (int)OOB, hw * (56 * 8192) + 32 * 8192, QAUX);
      ep = __builtin_amdgcn_raw_buffer_load_b64(rws, hw == 7 ? WS_E : OOB, 0, 16);
    }
    *(lds_v2f*)&L.ml[w][0] = v2f{0.f, -1.f};
    if (GOV) { *(lds_u*)&L.gate = t0 - (1u << 24); *(lds_u*)&L.lag[lane & 3] = t0; }
    wg_barrier();
    __builtin_amdgcn_sched_barrier(0);
  }

  if (w < 4) {
    const char* vb = vc + (size_t)bid * (1u << 20);
    auto so = [](int i) -> unsigned { return (unsigned)i * 4096u; };
    if (SPRIO) __builtin_amdgcn_s_setprio(SPRIO);
    unsigned tgate = t0 + PACE0;
    unsigned gnext = t0 - (1u << 24), kk = 0;
    auto pace = [&]() __attribute__((always_inline)) {
      if (GOV) {
        const unsigned g = __builtin_amdgcn_readfirstlane(gnext) + kk;
        unsigned now;
        while ((int)((now = (unsigned)__builtin_amdgcn_s_memrealtime()) - g) < 0) __builtin_amdgcn_s_sleep(2);
        *(lds_u*)&L.lag[w] = now - kk;
        gnext = *(lds_u*)&L.gate;
        kk += PN;
      } else if (PACE) {
        while ((int)((unsigned)__builtin_amdgcn_s_memrealtime() - tgate) < 0) __builtin_amdgcn_s_sleep(2);
        tgate += PACE;
      }
    };
    {
      const rsrc_t rk = mk(kb, 1u << 20);
#pragma unroll
      for (int i = PF; i < 32; ++i) {
        asm volatile("s_waitcnt vmcnt(%0)" ::"n"(PF - 1) : "memory");
        if (((i - PF) & 7) == 0) pace();
        a[i] = __builtin_amdgcn_raw_buffer_load_b128(rk, voff, so(i), 2);
      }
    }
    __builtin_amdgcn_sched_barrier(0);
    if (w == 0) { STAMPV(0, t0); STAMP(8); if (SH == 8) { STAMPV(13, ts[1]); STAMPV(14, ts[2]); STAMPV(15, ts[3]); } }
    wg_barrier();
    const v4u q = *(const v4u*)&L.q[l31 * 4];
    if (w == 0) STAMP(1);
    const bool b3 = lane & 8, b2 = lane & 4, b1 = lane & 2, b0 = lane & 1;
    const int rev = __builtin_bitreverse32(l31) >> 27;
    float* pw = &L.p[w][0][half][rev];
    float mx = -__builtin_inff();
    auto kround = [&](const int r, const bool first) __attribute__((always_inline)) {
      const rsrc_t rc = mk(kb, 1u << 20), rn = mk(r < 7 ? kb : vb, 1u << 20);
      const unsigned sc = (unsigned)r << 17, sn = (unsigned)((r + 1) & 7) << 17;
      float p0 = 0, p1 = 0, p2 = 0, p3 = 0, p4 = 0;
#pragma unroll
      for (int i = 0; i < 32; ++i) {
        auto refill = [&]() __attribute__((always_inline)) {
          if ((i & 7) == 0) pace();
          if (i + WD >= 32) a[(i + WD) & 31] = __builtin_amdgcn_raw_buffer_load_b128(rn, voff, sn + so((i + WD) & 31), 2);
          else a[i + WD] = __builtin_amdgcn_raw_buffer_load_b128(rc, voff, sc + so(i + WD), 2);
        };
        if (EARLY && !first) { asm volatile("" : "+v"(a[i])::"memory"); refill(); __builtin_amdgcn_sched_barrier(0); }
        float v = dot2(a[i][0], q[0], 0.f), v2 = dot2(a[i][1], q[1], 0.f);
        v = dot2(a[i][2], q[2], v); v2 = dot2(a[i][3], q[3], v2);
        v += v2;
        asm volatile("" : "+v"(v));
        if (first) {
          if (i == 0) pace();
          if (i < WD) a[i] = __builtin_amdgcn_raw_buffer_load_b128(rn, voff, sn + so(i), 2);
        } else if (!EARLY) refill();
        if (i & 1) { v = mrg16(p0, v);
          if (i & 2) { v = mrg<DPP_M>(p1, v, b3);
            if (i & 4) { v = mrg<DPP_HM>(p2, v, b2);
              if (i & 8) { v = mrg<DPP_X2>(p3, v, b1);
                if (i & 16) { v = rbf(mrg<DPP_X1>(p4, v, b0)) * (0.0625f * 1.44269504088896340736f); pw[r * 64] = v; mx = fmaxf(mx, v); }
                else p4 = v; }
              else p3 = v; }
            else p2 = v; }
          else p1 = v; }
        else p0 = v;
        __builtin_amdgcn_sched_barrier(0);
      }
    };
    kround(0, true);
#pragma unroll 1
    for (int r = 1; r < 8; ++r) { if (w == 0) STAMP(16 + r); kround(r, false); }
    if (w == 0) STAMP(2);
    {
      mx = wave_max(mx);
      float sum = 0;
#pragma unroll
      for (int r = 0; r < 8; ++r) { const float p = ex2(pw[r * 64] - mx); pw[r * 64] = p; sum += p; }
      sum = wave_sum(sum);
      if (lane == 0) *(lds_v2f*)&L.ml[w][0] = v2f{mx, sum};
    }
    v2f acc[4] = {{0, 0}, {0, 0}, {0, 0}, {0, 0}};
    const v4f* pr = (const v4f*)&L.p[w][0][half][0];
#pragma unroll 1
    for (int r = 0; r < 8; ++r) {
      const rsrc_t rc = mk(vb, 1u << 20), rn = mk(vb, r < 7 ? 1u << 20 : 0u);
      const unsigned sc = (unsigned)r << 17, sn = (unsigned)((r + 1) & 7) << 17;
      if (w == 0) STAMP(24 + r);
      v4f pv = pr[r * 16];
#pragma unroll
      for (int i = 0; i < 32; ++i) {
        auto refill = [&]() __attribute__((always_inline)) {
          if ((i & 7) == 0 && (i + WD < 32 || r < 7)) pace();
          if (i + WD >= 32) a[(i + WD) & 31] = __builtin_amdgcn_raw_buffer_load_b128(rn, voff, sn + so((i + WD) & 31), 2);
          else a[i + WD] = __builtin_amdgcn_raw_buffer_load_b128(rc, voff, sc + so(i + WD), 2);
        };
        if (EARLY) { asm volatile("" : "+v"(a[i])::"memory"); refill(); __builtin_amdgcn_sched_barrier(0); }
        const float p = pv[i & 3];
        if ((i & 3) == 3 && i < 31) pv = pr[r * 16 + (i + 1) / 4];
        const v2f pp = {p, p};
#pragma unroll
        for (int e = 0; e < 4; ++e) acc[e] = __builtin_elementwise_fma(unpk(a[i][e]), pp, acc[e]);
        asm volatile("" : "+v"(acc[0]), "+v"(acc[1]), "+v"(acc[2]), "+v"(acc[3]));
        if (!EARLY) refill();
        __builtin_amdgcn_sched_barrier(0);
      }
    }
    {
      v4u hi, lo;
#pragma unroll
      for (int e = 0; e < 4; ++e) {
        hi[e] = pkbf(acc[e][0], acc[e][1]);
        const v2f r = acc[e] - unpk(hi[e]);
        lo[e] = pkbf(r[0], r[1]);
      }
      char* ap = L.a + (w * 4 + half * 2) * AROW + l31 * 16;
      *(v4u*)ap = hi;
      *(v4u*)(ap + AROW) = lo;
    }
    if (w == 0) STAMP(3);
    wg_barrier();
    asm volatile("s_endpgm" : "=v"(a[0]), "=v"(a[1]), "=v"(a[2]), "=v"(ep)::"memory");
  }

  unsigned tg = t0 + (hw < 7 ? WO0 + hw * WOS : GL0);
  auto gate = [&](unsigned d) __attribute__((always_inline)) {
    while ((int)((unsigned)__builtin_amdgcn_s_memrealtime() - tg) < 0) __builtin_amdgcn_s_sleep(4);
    tg += d;
  };
  v4u f0 = a[0];
  TNOW(t13);
  asm volatile("" : "+v"(f0));
  __builtin_amdgcn_sched_barrier(0);
  TNOW(t14);
  {
    v2f xv[4];
#pragma unroll
    for (int e = 0; e < 4; ++e) xv[e] = unpk(f0[e]);
    const v2f s2 = (xv[0] + xv[1]) + (xv[2] + xv[3]);
    const float mean = wave_sum(s2[0] + s2[1]) * (1.0f / D_);
    float ss = 0;
#pragma unroll
    for (int e = 0; e < 4; ++e) { const v2f d = xv[e] - mean; ss += d[0] * d[0] + d[1] * d[1]; }
    ss = lane < 56 ? ss : 0.f;
    const float mb = rbf(mean);
    const float rs = __builtin_amdgcn_rcpf(rbf(__builtin_amdgcn_sqrtf(rbf(rbf(wave_sum(ss) * (1.0f / D_)) + 1e-5f))));
    v4u xb;
#pragma unroll
    for (int e = 0; e < 4; ++e) { const v2f d = unpk(pkbf(xv[e][0] - mb, xv[e][1] - mb)) * rs; xb[e] = pkbf(d[0], d[1]); }
    if (lane < 56) {
      *(v4u*)(L.xa + hw * XROW + lane * 16) = xb;
      if (hw == b) {
        v4f* p = (v4f*)&L.xn[lane * 8];
        const v2f c0 = unpk(xb[0]), c1 = unpk(xb[1]), c2 = unpk(xb[2]), c3 = unpk(xb[3]);
        p[0] = v4f{c0[0], c0[1], c1[0], c1[1]};
        p[1] = v4f{c2[0], c2[1], c3[0], c3[1]};
      }
    }
    TNOW(t15);
    *(v4u*)(L.wqs + hw * (56 * 32) + lane * 16) = a[1];
    if (lane < 48) *(v4u*)(L.wqs + hw * (56 * 32) + 1024 + lane * 16) = a[2];
    asm volatile("s_waitcnt lgkmcnt(0)" ::: "memory");
    *(lds_u*)&L.ml[w][0] = 1u;
  }
  TNOW(t9);
  const unsigned tlo = ep[0] + 0x01010101u, thi = ep[1] + 0x01010101u;
  if (hw == 7 && !(ABL & 1)) {
#pragma unroll 1
    for (unsigned spin = 0; spin < SPIN_MAX; ++spin)
      if (__builtin_amdgcn_ballot_w64(*(lds_u*)&L.ml[4 + (lane & 7)][0] == 0u) == 0) break;
    asm volatile("" ::: "memory");
    TNOW(t10);
    const int kg = lane >> 4, nn = lane & 15;
    typedef __attribute__((address_space(3))) v4s lds_v4s;
    const char* ap = L.xa + (nn & 7) * XROW + kg * 16;
    const char* bp = L.wqs + kg * 256 + nn * 8;
    v4f d0 = {0, 0, 0, 0}, d1 = {0, 0, 0, 0};
#pragma unroll
    for (int ks = 0; ks < 14; ++ks) {
      const v8bf af = *(const v8bf*)(ap + ks * 64);
      const v4s b0 = __builtin_amdgcn_ds_read_tr16_b64_v4i16((lds_v4s*)(bp + ks * 1024));
      const v4s b1 = __builtin_amdgcn_ds_read_tr16_b64_v4i16((lds_v4s*)(bp + ks * 1024 + 128));
      const v8bf bfr = __builtin_bit_cast(v8bf, __builtin_shufflevector(b0, b1, 0, 1, 2, 3, 4, 5, 6, 7));
      if (ks & 1) d1 = __builtin_amdgcn_mfma_f32_16x16x32_bf16(af, bfr, d1, 0, 0, 0);
      else d0 = __builtin_amdgcn_mfma_f32_16x16x32_bf16(af, bfr, d0, 0, 0, 0);
    }
    d0 += d1;
    const bool odd = nn & 1;
    const float u0 = kg < 2 ? d0[0] : d0[2], u1 = kg < 2 ? d0[1] : d0[3];
    const float keep = odd ? u1 : u0, recv = dpp<DPP_X1>(odd ? u0 : u1);
    const unsigned vs = WS_Q + qg * 512 + ((kg & 1) * 4 + (kg >> 1) * 2 + (nn & 1)) * 64 + (nn >> 1) * 8;
    __builtin_amdgcn_raw_buffer_store_b64(v2u{pkbf(odd ? recv : keep, odd ? keep : recv), (nn & 2) ? thi : tlo}, rws, vs, 0, 16);
    TNOW(t11);
    *(v2u*)L.tag = v2u{tlo, thi};
    if (QW) asm volatile("s_waitcnt vmcnt(0)" ::: "memory");
    TNOW(t5);
    if (QG) while ((int)((unsigned)__builtin_amdgcn_s_memrealtime() - (t0 + QG)) < 0) __builtin_amdgcn_s_sleep(2);
    v4u g;
#pragma unroll 1
    for (unsigned spin = 0; spin < (NOQ ? 1 : SPIN_MAX); ++spin) {
      asm volatile("" ::: "memory");
      g = __builtin_amdgcn_raw_buffer_load_b128(rws, WS_Q + (h * 16 + (lane >> 2)) * 512 + b * 64 + (lane & 3) * 16, 0, 16);
      if (__builtin_amdgcn_ballot_w64(g[1] != tlo || g[3] != thi) == 0) break;
      if (QS) __builtin_amdgcn_s_sleep(QS);
    }
    *(v2u*)&L.q[lane * 2] = v2u{g[0], g[2]};
    STAMP(12); STAMPV(11, t11); STAMPV(10, t10); STAMPV(5, t5);
  }
  wg_barrier();
  if (hw == SH) { STAMPV(9, t9); STAMPV(13, t13); STAMPV(14, t14); STAMPV(15, t15_); }

  if (ABL & 2) { wg_barrier(); STAMP(7); return; }
  if (hw < 7) {
    const int kg = lane >> 4, nn = lane & 15;
    const rsrc_t ro = mk(wo + (size_t)h * (DH_ * D_ * 2), NO_WO == 1 ? 0 : DH_ * D_ * 2);
    const int vo = kg * 8 * (D_ * 2) + hw * 128 + nn * 8;
    v4u bf[8][4];
    const bool gov = (GOV >> hw) & 1;
    const unsigned gtag = L.tag[0], vp = WS_P + (hw * 8 + b) * 256;
    const unsigned ytag = L.tag[nn & 1];
#pragma unroll
    for (int ks = 0; ks < 8; ++ks) {
      v2u t[8];
      if (WO0) gate(gov ? GPD : WOD);
      if (gov) {
        int d = (int)(*(lds_u*)&L.lag[lane & 3] - t0);
        d += __builtin_amdgcn_update_dpp(0, d, DPP_X1, 0xf, 0xf, true);
        d += __builtin_amdgcn_update_dpp(0, d, DPP_X2, 0xf, 0xf, true);
        __builtin_amdgcn_raw_buffer_store_b64(v2u{t0 + (unsigned)(d >> 2), gtag}, rws, lane == 0 ? vp + j * 8 : OOB, 0, 16);
      }
#pragma unroll
      for (int e = 0; e < 8; ++e) t[e] = (NO_WO == 2 || (NO_WO == 3 && ks >= 4)) ? v2u{(unsigned)vo, (unsigned)e} : __builtin_amdgcn_raw_buffer_load_b64(ro, vo, (ks * 32 + e) * (D_ * 2), WAUX);
#pragma unroll
      for (int d = 0; d < 4; ++d) {
        bf[ks][0][d] = __builtin_amdgcn_perm(t[2 * d + 1][0], t[2 * d][0], 0x05040100u);
        bf[ks][1][d] = __builtin_amdgcn_perm(t[2 * d + 1][0], t[2 * d][0], 0x07060302u);
        bf[ks][2][d] = __builtin_amdgcn_perm(t[2 * d + 1][1], t[2 * d][1], 0x05040100u);
        bf[ks][3][d] = __builtin_amdgcn_perm(t[2 * d + 1][1], t[2 * d][1], 0x07060302u);
      }
      asm volatile("" : "+v"(bf[ks][0]), "+v"(bf[ks][1]), "+v"(bf[ks][2]), "+v"(bf[ks][3]));
      __builtin_amdgcn_sched_barrier(0);
      if (gov) {
        gate(WOD - GPD);
        const v2u g = __builtin_amdgcn_raw_buffer_load_b64(rws, vp + l31 * 8, 0, 16);
        const bool ok = g[1] == gtag;
        const float d = wave_sum(ok ? (float)min(max((int)(g[0] - t0), -2000), 2000) : 0.f);
        const float n = (float)__builtin_popcountll(__builtin_amdgcn_ballot_w64(ok));
        if (n > 0.f) *(lds_u*)&L.gate = t0 + (unsigned)((int)(d * __builtin_amdgcn_rcpf(n)) - DELTA);
      }
    }
    wg_barrier();
    const char* ap = L.a + nn * AROW + kg * 16;
    v4f dd[4] = {{0, 0, 0, 0}, {0, 0, 0, 0}, {0, 0, 0, 0}, {0, 0, 0, 0}};
    const v4f m01 = *(const v4f*)&L.ml[0][0], m23 = *(const v4f*)&L.ml[2][0];
    const float mk_ = L.ml[kg][0], wgt = L.wgt;
    const v4f zz = *(const v4f*)&L.z[hw * 64 + nn * 4];
#pragma unroll
    for (int ks = 0; ks < 8; ++ks) {
      const v8bf af = *(const v8bf*)(ap + ks * 64);
#pragma unroll
      for (int t = 0; t < 4; ++t)
        dd[t] = __builtin_amdgcn_mfma_f32_16x16x32_bf16(af, __builtin_bit_cast(v8bf, bf[ks][t]), dd[t], 0, 0, 0);
    }
    const float sc = ex2(mk_ - fmaxf(fmaxf(m01[0], m01[2]), fmaxf(m23[0], m23[2]))) * wgt;
    v4u yb;
#pragma unroll
    for (int t = 0; t < 4; ++t) {
      float y = ((dd[t][0] + dd[t][1]) + (dd[t][2] + dd[t][3])) * sc;
      y = mrg16(y, y);
      y = mrg32(y, y) + zz[t];
      yb[t] = (asu(y) & 0xffffff00u) | ((ytag >> (8 * t)) & 0xffu);
    }
    __builtin_amdgcn_raw_buffer_store_b128(yb, rws, lane < 16 ? WS_Y + bid * SLOT + hw * 256 + nn * 16 : OOB, 0, 16);
    if (hw == 0) STAMP(4);
    return;
  }

  v2f z[4] = {{0, 0}, {0, 0}, {0, 0}, {0, 0}};
  if (gs < 28 && NO_GLU != 2) {
    const int rg = lane >> 3;
    const rsrc_t rgt = mk(wg, NO_GLU ? 0 : D_ * F_ * 2), rup = mk(wu, NO_GLU ? 0 : D_ * F_ * 2);
    const int vo = rg * 56 * (F_ * 2) + gs * 128 + (lane & 7) * 16;
    v2f g[4] = {{0, 0}, {0, 0}, {0, 0}, {0, 0}}, u[4] = {{0, 0}, {0, 0}, {0, 0}, {0, 0}};
    v4u ta[4], tb[4];
    auto ldgu = [&](v4u* t, int c) __attribute__((always_inline)) {
      gate(GLD);
#pragma unroll
      for (int i = 0; i < 2; ++i) {
        t[i] = __builtin_amdgcn_raw_buffer_load_b128(rgt, vo, (c * 2 + i) * (F_ * 2), WAUX);
        t[i + 2] = __builtin_amdgcn_raw_buffer_load_b128(rup, vo, (c * 2 + i) * (F_ * 2), WAUX);
      }
      __builtin_amdgcn_sched_barrier(0);
    };
    auto usegu = [&](const v4u* t, float x0, float x1) __attribute__((always_inline)) {
#pragma unroll
      for (int e = 0; e < 4; ++e) {
        g[e] = __builtin_elementwise_fma(unpk(t[0][e]), v2f{x0, x0}, g[e]);
        g[e] = __builtin_elementwise_fma(unpk(t[1][e]), v2f{x1, x1}, g[e]);
        u[e] = __builtin_elementwise_fma(unpk(t[2][e]), v2f{x0, x0}, u[e]);
        u[e] = __builtin_elementwise_fma(unpk(t[3][e]), v2f{x1, x1}, u[e]);
      }
      asm volatile("" : "+v"(g[0]), "+v"(g[1]), "+v"(g[2]), "+v"(g[3]), "+v"(u[0]), "+v"(u[1]), "+v"(u[2]), "+v"(u[3]));
      __builtin_amdgcn_sched_barrier(0);
    };
    ldgu(ta, 0);
#pragma unroll 1
    for (int c = 0; c < 28; c += 2) {
      const v4f xx = *(const v4f*)&L.xn[rg * 56 + c * 2];
      ldgu(tb, c + 1);
      usegu(ta, xx[0], xx[1]);
      ldgu(ta, min(c + 2, 27));
      usegu(tb, xx[2], xx[3]);
    }
    const rsrc_t rd = mk(wd + gs * (64 * D_ * 2), NO_GLU ? 0 : 64 * D_ * 2);
    const unsigned vd = lane < 56 ? (unsigned)lane * 16u : OOB;
    auto ldd = [&](v4u* t, int c) __attribute__((always_inline)) {
      gate(GLD);
#pragma unroll
      for (int i = 0; i < 4; ++i) t[i] = __builtin_amdgcn_raw_buffer_load_b128(rd, vd, (c * 4 + i) * (D_ * 2), WAUX);
      __builtin_amdgcn_sched_barrier(0);
    };
    ldd(ta, 0);
    float hv[8];
#pragma unroll
    for (int e = 0; e < 8; ++e) {
      float gg = g[e >> 1][e & 1], uu = u[e >> 1][e & 1];
      gg += dpp<DPP_R8>(gg); gg = mrg16(gg, gg); gg = mrg32(gg, gg);
      uu += dpp<DPP_R8>(uu); uu = mrg16(uu, uu); uu = mrg32(uu, uu);
      hv[e] = rbf(rbf(1.0f / (1.0f + expf(-rbf(gg)))) * rbf(uu));
    }
    if (lane < 8) {
      *(v4f*)&L.hv[lane * 8] = v4f{hv[0], hv[1], hv[2], hv[3]};
      *(v4f*)&L.hv[lane * 8 + 4] = v4f{hv[4], hv[5], hv[6], hv[7]};
    }
    auto used = [&](const v4u* t, const v4f hh) __attribute__((always_inline)) {
#pragma unroll
      for (int k = 0; k < 4; ++k)
#pragma unroll
        for (int e = 0; e < 4; ++e) z[e] = __builtin_elementwise_fma(unpk(t[k][e]), v2f{hh[k], hh[k]}, z[e]);
      asm volatile("" : "+v"(z[0]), "+v"(z[1]), "+v"(z[2]), "+v"(z[3]));
      __builtin_amdgcn_sched_barrier(0);
    };
#pragma unroll 1
    for (int c = 0; c < 16; c += 2) {
      const v4f h0 = *(const v4f*)&L.hv[c * 4], h1 = *(const v4f*)&L.hv[c * 4 + 4];
      ldd(tb, c + 1);
      used(ta, h0);
      ldd(ta, min(c + 2, 15));
      used(tb, h1);
    }
  }
  if (lane < 56) {
    *(v4f*)&L.z[lane * 8] = v4f{z[0][0], z[0][1], z[1][0], z[1][1]};
    *(v4f*)&L.z[lane * 8 + 4] = v4f{z[2][0], z[2][1], z[3][0], z[3][1]};
  }
  {
    v2f ml;
#pragma unroll 1
    for (unsigned spin = 0; spin < SPIN_MAX; ++spin) {
      ml = *(lds_v2f*)&L.ml[lane & 3][0];
      if (__builtin_amdgcn_ballot_w64(asu(ml[1]) == 0xBF800000u) == 0) break;
      __builtin_amdgcn_s_sleep(16);
    }
    float m = fmaxf(ml[0], dpp<DPP_X1>(ml[0]));
    m = fmaxf(m, dpp<DPP_X2>(m));
    float l = ml[1] * ex2(ml[0] - m);
    l += dpp<DPP_X1>(l);
    l += dpp<DPP_X2>(l);
    const v2u gr = (lane & 1) ? v2u{asu(l), thi} : v2u{asu(m), tlo};
    __builtin_amdgcn_raw_buffer_store_b64(gr, rws, lane < 2 ? WS_REC + bid * 128 + lane * 8 : OOB, 0, 16);
    v4u g;
#pragma unroll 1
    for (unsigned spin = 0; spin < SPIN_MAX; ++spin) {
      asm volatile("" ::: "memory");
      g = __builtin_amdgcn_raw_buffer_load_b128(rws, WS_REC + (bid ^ 1) * 128, 0, 16);
      if (__builtin_amdgcn_ballot_w64(g[1] != tlo || g[3] != thi) == 0) break;
      __builtin_amdgcn_s_sleep(64);
      if (ABL & 8) break;
    }
    const float mo = asf(g[0]), M = fmaxf(m, mo), e = ex2(m - M), eo = ex2(mo - M);
    const float la = l * e, lb = asf(g[2]) * eo;
    L.wgt = e / ((j & 1) ? lb + la : la + lb);
  }
  if (j >= 28) { wg_barrier(); return; }
  const unsigned vy = (b * 32 + l31) * SLOT + j * 64 + half * 32;
  const int col = j * 16 + half * 8 + ((lane >> 4) & 1) + ((lane >> 2) & 2) + (lane & 4);
  const float xres = asf((unsigned)__builtin_amdgcn_raw_buffer_load_b16(mk(x + b * (D_ * 2), D_ * 2), col * 2, 0, 0) << 16);
  const bool b3 = lane & 8, b2 = lane & 4;
  auto red = [&](const float* v) -> float {
    float r = mrg<DPP_HM>(mrg<DPP_M>(mrg16(v[0], v[1]), mrg16(v[2], v[3]), b3), mrg<DPP_M>(mrg16(v[4], v[5]), mrg16(v[6], v[7]), b3), b2);
    r += dpp<DPP_X2>(r);
    r += dpp<DPP_X1>(r);
    return r;
  };
  STAMP(6);
  wg_barrier();
  v4u y0, y1;
#pragma unroll 1
  for (unsigned spin = 0; spin < SPIN_MAX; ++spin) {
    asm volatile("" ::: "memory");
    y0 = __builtin_amdgcn_raw_buffer_load_b128(rws, WS_Y + vy, 0, 16);
    y1 = __builtin_amdgcn_raw_buffer_load_b128(rws, WS_Y + vy + 16, 0, 16);
    const unsigned c0 = __builtin_amdgcn_perm(__builtin_amdgcn_perm(y0[3], y0[2], 0x0400u), __builtin_amdgcn_perm(y0[1], y0[0], 0x0400u), 0x05040100u);
    const unsigned c1 = __builtin_amdgcn_perm(__builtin_amdgcn_perm(y1[3], y1[2], 0x0400u), __builtin_amdgcn_perm(y1[1], y1[0], 0x0400u), 0x05040100u);
    if (__builtin_amdgcn_ballot_w64(c0 != tlo || c1 != thi) == 0) break;
    __builtin_amdgcn_s_sleep(2);
    if (ABL & 16) break;
  }
  float v[8];
#pragma unroll
  for (int e = 0; e < 4; ++e) { v[e] = asf(y0[e] & 0xffffff00u); v[e + 4] = asf(y1[e] & 0xffffff00u); }
  const unsigned ob = pkbf(xres + red(v), 0.f);
  __builtin_amdgcn_raw_buffer_store_b16((unsigned short)ob, mk(out + b * (D_ * 2), D_ * 2), (lane & 3) == 0 ? (unsigned)col * 2u : OOB, 0, 0);
  if (bid == 0) __builtin_amdgcn_raw_buffer_store_b64(v2u{tlo, thi}, rws, lane == 0 ? WS_E : OOB, 0, 16);
  STAMP(7);
}

extern "C" void kernel_launch(void* const* d_in, const int* in_sizes, int n_in, void* d_out, int out_size,
                              void* d_ws, size_t ws_size, hipStream_t stream) {
  (void)in_sizes; (void)n_in; (void)out_size; (void)ws_size;
  fused_decode_kernel<<<256, 768, 0, stream>>>((const char*)d_in[0], (const char*)d_in[1], (const char*)d_in[3], (char*)d_ws,
                                               (const char*)d_in[2], (const char*)d_in[4], (const char*)d_in[6],
                                               (const char*)d_in[7], (const char*)d_in[8], (char*)d_out);
}
```

```cpp
#include <hip/hip_runtime.h>
#include <stdint.h>

typedef unsigned v4u __attribute__((ext_vector_type(4)));
typedef unsigned v2u __attribute__((ext_vector_type(2)));
typedef float v4f __attribute__((ext_vector_type(4)));
typedef float v2f __attribute__((ext_vector_type(2)));
typedef __bf16 v8bf __attribute__((ext_vector_type(8)));
typedef __bf16 v2bf __attribute__((ext_vector_type(2)));
typedef __amdgpu_buffer_rsrc_t rsrc_t;
typedef __attribute__((address_space(3))) volatile v2f lds_v2f;
typedef __attribute__((address_space(3))) volatile unsigned lds_u;
typedef short v4s __attribute__((ext_vector_type(4)));
typedef short v8s __attribute__((ext_vector_type(8)));

#define NX 4
#define MAP 5
#define WO0 800
#define WOD 700
#define WOS 100
#define GL0 750
#define GLD 110
#define SH 0
#define WX 8
#define LIM 5
#define GOV 0x11
#define PUB 0x09
#define AGG 0x24
#define RST 512
#define PN 116
#define DELTA 20
#define SMX 40
#define GINIT (1u << 24)

namespace {

constexpr int D_ = 448, DH_ = 256, F_ = 1792;
constexpr unsigned WS_E = 0, WS_P = 4096, WS_REC = 32768, WS_Q = 65536, WS_Y = 256u << 10, WS_G = 1u << 20, WS_SPAN = 2u << 20;
constexpr unsigned SLOT = D_ * 4;
constexpr unsigned OOB = 0x80000000u;
constexpr unsigned SPIN_MAX = 1u << 22;
constexpr int WD = 8;
constexpr int NT = 2, SC1 = 16;
constexpr int AROW = 528;
constexpr int XROW = 912;

struct __attribute__((aligned(16))) Lds {
  float xn[D_];
  char xa[8 * XROW];
  char wqs[D_ * 32];
  float z[D_];
  unsigned q[DH_ / 2];
  unsigned tag[2];
  float wgt;
  unsigned gate;
  unsigned lag[4];
  float p[4][8][2][32];
  float hv[64];
  float ml[12][2];
  char a[16 * AROW];
  v4u kx[4][NX ? NX : 1][64];
};

__device__ __forceinline__ rsrc_t mk(const void* p, unsigned n) {
  return __builtin_amdgcn_make_buffer_rsrc((void*)p, 0, n, 0x00020000);
}
__device__ __forceinline__ float asf(unsigned u) { return __builtin_bit_cast(float, u); }
__device__ __forceinline__ unsigned asu(float f) { return __builtin_bit_cast(unsigned, f); }
__device__ __forceinline__ v2f unpk(unsigned d) { return v2f{asf(d << 16), asf(d & 0xffff0000u)}; }
template <int C> __device__ __forceinline__ float dpp(float v) {
  return asf(__builtin_amdgcn_update_dpp(0, asu(v), C, 0xf, 0xf, true));
}
constexpr int DPP_X1 = 0xB1, DPP_X2 = 0x4E, DPP_HM = 0x141, DPP_M = 0x140, DPP_R8 = 0x128;
__device__ __forceinline__ float mrg16(float a, float b) {
  auto r = __builtin_amdgcn_permlane16_swap(asu(a), asu(b), false, false);
  return asf(r[0]) + asf(r[1]);
}
__device__ __forceinline__ float mrg32(float a, float b) {
  auto r = __builtin_amdgcn_permlane32_swap(asu(a), asu(b), false, false);
  return asf(r[0]) + asf(r[1]);
}
template <int C> __device__ __forceinline__ float mrg(float a, float b, bool hi) {
  const float keep = hi ? b : a, send = hi ? a : b;
  return keep + dpp<C>(send);
}
__device__ __forceinline__ float wave_sum(float v) {
  v += dpp<DPP_X1>(v); v += dpp<DPP_X2>(v); v += dpp<DPP_HM>(v); v += dpp<DPP_M>(v);
  v = mrg16(v, v); v = mrg32(v, v);
  return v;
}
__device__ __forceinline__ float wave_max(float v) {
  v = fmaxf(v, dpp<DPP_X1>(v)); v = fmaxf(v, dpp<DPP_X2>(v));
  v = fmaxf(v, dpp<DPP_HM>(v)); v = fmaxf(v, dpp<DPP_M>(v));
  auto r = __builtin_amdgcn_permlane16_swap(asu(v), asu(v), false, false);
  v = fmaxf(asf(r[0]), asf(r[1]));
  r = __builtin_amdgcn_permlane32_swap(asu(v), asu(v), false, false);
  return fmaxf(asf(r[0]), asf(r[1]));
}
__device__ __forceinline__ unsigned pkbf(float a, float b) {
  return __builtin_bit_cast(unsigned, __builtin_convertvector(v2f{a, b}, v2bf));
}
__device__ __forceinline__ float rbf(float a) { return asf(pkbf(a, 0.f) << 16); }
__device__ __forceinline__ float dot2(unsigned a, unsigned b, float c) {
  return __builtin_amdgcn_fdot2_f32_bf16(__builtin_bit_cast(v2bf, a), __builtin_bit_cast(v2bf, b), c, false);
}
__device__ __forceinline__ void wg_barrier() { asm volatile("s_waitcnt lgkmcnt(0)\n\ts_barrier" ::: "memory"); }
__device__ __forceinline__ float ex2(float x) { return __builtin_amdgcn_exp2f(x); }

#define TNOW(v) do { } while (0)
#define STAMP(k) do { } while (0)
#define STAMPV(k, v) do { } while (0)

}

__global__ void __launch_bounds__(768, 3)
fused_decode_kernel(const char* __restrict__ x, const char* __restrict__ kc, const char* __restrict__ wq, char* ws,
                    const char* __restrict__ vc, const char* __restrict__ wo, const char* __restrict__ wg,
                    const char* __restrict__ wu, const char* __restrict__ wd, char* out) {
  __shared__ Lds L;
  asm volatile("" ::"s"(x), "s"(kc), "s"(wq), "s"(ws));
  const int lane = threadIdx.x & 63;
  const int w = 11 - __builtin_amdgcn_readfirstlane(threadIdx.x >> 6);
  const int bx = blockIdx.x, b = bx >> 5, gs = bx & 31;
  const int j = (bx & 7) * 2 + ((bx >> 3) & 1) + (bx & 16);
  const int h = j >> 1, bid = b * 32 + j;
  const int half = lane >> 5, l31 = lane & 31;
  const int hw = w - 4;
  const unsigned t0 = (unsigned)__builtin_amdgcn_s_memrealtime();
  const char* kb = kc + (size_t)bid * (1u << 20);
  const int voff = w * 1024 + lane * 16;
  const int qg = j * 8 + b;
  const rsrc_t rws = mk(ws, WS_SPAN);
  v4u a[32];
  v2u ep;
  {
    if (w < 4) {
      const rsrc_t rk = mk(kb, 1u << 20);
#pragma unroll
      for (int i = 0; i < WD; ++i) a[i] = __builtin_amdgcn_raw_buffer_load_b128(rk, voff, i * 4096, NT);
    } else {
      const rsrc_t r1 = mk(wq + qg * 32, 447 * 8192 + 32);
      const int vq = (lane >> 1) * 8192 + (lane & 1) * 16;
      a[0] = __builtin_amdgcn_raw_buffer_load_b128(mk(x + hw * (D_ * 2), D_ * 2), lane * 16, 0, 0);
      a[1] = __builtin_amdgcn_raw_buffer_load_b128(r1, vq, hw * (56 * 8192), NT);
      a[2] = __builtin_amdgcn_raw_buffer_load_b128(r1, lane < 48 ? vq : (int)OOB, hw * (56 * 8192) + 32 * 8192, NT);
      ep = __builtin_amdgcn_raw_buffer_load_b64(rws, hw == 7 ? WS_E : OOB, 0, SC1);
    }
    *(lds_v2f*)&L.ml[w][0] = v2f{0.f, -1.f};
    *(lds_u*)&L.gate = t0 + GINIT;
    *(lds_u*)&L.lag[lane & 3] = t0 + 300;
    wg_barrier();
    __builtin_amdgcn_sched_barrier(0);
  }

  if (w < 4) {
    const char* vb = vc + (size_t)bid * (1u << 20);
    auto so = [](int i) -> unsigned { return (unsigned)i * 4096u; };
    __builtin_amdgcn_s_setprio(3);
    {
      const rsrc_t rk = mk(kb, 1u << 20);
#pragma unroll
      for (int i = WD; i < 32; ++i) {
        asm volatile("s_waitcnt vmcnt(%0)" ::"n"(WD - 1) : "memory");
        a[i] = __builtin_amdgcn_raw_buffer_load_b128(rk, voff, so(i), NT);
      }
#pragma unroll
      for (int i = 0; i < NX; ++i) {
        asm volatile("s_waitcnt vmcnt(%0)" ::"n"(WD - 1) : "memory");
        __builtin_amdgcn_raw_ptr_buffer_load_lds(rk, (__attribute__((address_space(3))) void*)&L.kx[w][i][0], 16, voff, so(32 + i), 0, NT);
      }
    }
    __builtin_amdgcn_sched_barrier(0);
    if (w == 0) { STAMPV(0, t0); STAMP(8); }
    wg_barrier();
    v4u q;
    asm volatile("ds_read_b128 %0, %1\n\ts_waitcnt lgkmcnt(0)" : "=v"(q) : "v"((unsigned)(size_t)(lds_u*)&L.q[l31 * 4]) : "memory");
    if (w == 0) STAMP(1);
    const bool b3 = lane & 8, b2 = lane & 4, b1 = lane & 2, b0 = lane & 1;
    const int rev = __builtin_bitreverse32(l31) >> 27;
    float* pw = &L.p[w][0][half][rev];
    float mx = -__builtin_inff();
    unsigned gnext = t0 + GINIT, kk = 7 * PN;
    int deep = LIM < WD ? 1 : -1;
    auto pace = [&](const bool force) __attribute__((always_inline)) {
      const unsigned lag = (unsigned)__builtin_amdgcn_s_memrealtime() - kk;
      deep = force ? 1 : (int)(lag - __builtin_amdgcn_readfirstlane(gnext));
      *(lds_u*)&L.lag[w] = lag;
      gnext = *(lds_u*)&L.gate;
      kk += PN;
    };
    auto limit = [&]() __attribute__((always_inline)) {
      if (WX > LIM) asm volatile("s_cmp_gt_i32 %0, 0\n\ts_cbranch_scc1 1f\n\ts_waitcnt vmcnt(%1)\n1:" ::"s"(deep), "n"(LIM - 1) : "scc");
    };
    auto kround = [&](const int r, const int mode) __attribute__((always_inline)) {
      const rsrc_t rc = mk(kb, 1u << 20), rn = mk(r < 7 ? kb : vb, 1u << 20);
      const unsigned sc = (unsigned)r << 17, sn = (unsigned)((r + 1) & 7) << 17;
      const int sh = mode == 1 ? NX : 0;
      float p0 = 0, p1 = 0, p2 = 0, p3 = 0, p4 = 0;
      if (mode == 1) {
        asm volatile("s_waitcnt vmcnt(%0)" ::"n"(WX) : "memory");
#pragma unroll
        for (int i = 0; i < NX; ++i) a[32 - NX + i] = L.kx[w][i][lane];
      }
#pragma unroll
      for (int i = 0; i < 32; ++i) {
        const v4u c = a[(i - sh) & 31];
        float v = dot2(c[0], q[0], 0.f), v2 = dot2(c[1], q[1], 0.f);
        v = dot2(c[2], q[2], v); v2 = dot2(c[3], q[3], v2);
        v += v2;
        asm volatile("" : "+v"(v));
        if (mode == 0) {
          if (i < WD) a[i] = __builtin_amdgcn_raw_buffer_load_b128(rn, voff, sn + so(NX + i), NT);
          if (i >= 32 - (WX - WD)) a[i - 32 + WX] = __builtin_amdgcn_raw_buffer_load_b128(rn, voff, sn + so(NX + i - 32 + WX), NT);
        } else if (i >= sh) {
          if (mode == 2 && (i & 7) == 0) pace(false);
          limit();
          if (i + WX >= 32) a[(i + WX) & 31] = __builtin_amdgcn_raw_buffer_load_b128(rn, voff, sn + so((i + WX) & 31), NT);
          else a[i + WX - sh] = __builtin_amdgcn_raw_buffer_load_b128(rc, voff, sc + so(i + WX), NT);
        }
        if (i & 1) { v = mrg16(p0, v);
          if (i & 2) { v = mrg<DPP_M>(p1, v, b3);
            if (i & 4) { v = mrg<DPP_HM>(p2, v, b2);
              if (i & 8) { v = mrg<DPP_X2>(p3, v, b1);
                if (i & 16) { v = rbf(mrg<DPP_X1>(p4, v, b0)) * (0.0625f * 1.44269504088896340736f); pw[r * 64] = v; mx = fmaxf(mx, v); }
                else p4 = v; }
              else p3 = v; }
            else p2 = v; }
          else p1 = v; }
        else p0 = v;
        __builtin_amdgcn_sched_barrier(0);
      }
    };
    kround(0, 0);
    if (NX) { if (w == 0) STAMP(17); kround(1, 1); }
#pragma unroll 1
    for (int r = NX ? 2 : 1; r < 8; ++r) { if (w == 0) STAMP(16 + r); kround(r, 2); }
    if (w == 0) STAMP(2);
    {
      mx = wave_max(mx);
      float sum = 0;
#pragma unroll
      for (int r = 0; r < 8; ++r) { const float p = ex2(pw[r * 64] - mx); pw[r * 64] = p; sum += p; }
      sum = wave_sum(sum);
      if (lane == 0) *(lds_v2f*)&L.ml[w][0] = v2f{mx, sum};
    }
    kk += SMX;
    v2f acc[4] = {{0, 0}, {0, 0}, {0, 0}, {0, 0}};
    const v4f* pr = (const v4f*)&L.p[w][0][half][0];
#pragma unroll 1
    for (int r = 0; r < 8; ++r) {
      const rsrc_t rc = mk(vb, 1u << 20), rn = mk(vb, r < 7 ? 1u << 20 : 0u);
      const unsigned sc = (unsigned)r << 17, sn = (unsigned)((r + 1) & 7) << 17;
      if (w == 0) STAMP(24 + r);
      v4f pv = pr[r * 16];
#pragma unroll
      for (int i = 0; i < 32; ++i) {
        auto refill = [&]() __attribute__((always_inline)) {
          if ((i & 7) == 0) pace(i >= 16 && r == 7);
          limit();
          if (i + WX >= 32) a[(i + WX) & 31] = __builtin_amdgcn_raw_buffer_load_b128(rn, voff, sn + so((i + WX) & 31), NT);
          else a[i + WX] = __builtin_amdgcn_raw_buffer_load_b128(rc, voff, sc + so(i + WX), NT);
        };
        const float p = pv[i & 3];
        if ((i & 3) == 3 && i < 31) pv = pr[r * 16 + (i + 1) / 4];
        const v2f pp = {p, p};
#pragma unroll
        for (int e = 0; e < 4; ++e) acc[e] = __builtin_elementwise_fma(unpk(a[i][e]), pp, acc[e]);
        asm volatile("" : "+v"(acc[0]), "+v"(acc[1]), "+v"(acc[2]), "+v"(acc[3]));
        refill();
        __builtin_amdgcn_sched_barrier(0);
      }
    }
    {
      v4u hi, lo;
#pragma unroll
      for (int e = 0; e < 4; ++e) {
        hi[e] = pkbf(acc[e][0], acc[e][1]);
        const v2f r = acc[e] - unpk(hi[e]);
        lo[e] = pkbf(r[0], r[1]);
      }
      char* ap = L.a + (w * 4 + half * 2) * AROW + l31 * 16;
      *(v4u*)ap = hi;
      *(v4u*)(ap + AROW) = lo;
    }
    if (w == 0) STAMP(3);
    wg_barrier();
    asm volatile("s_endpgm" : "=v"(a[0]), "=v"(a[1]), "=v"(a[2]), "=v"(ep)::"memory");
  }

  unsigned tg = t0 + (hw < 7 ? WO0 + hw * WOS : GL0);
  auto gate = [&](unsigned d) __attribute__((always_inline)) {
    while ((int)((unsigned)__builtin_amdgcn_s_memrealtime() - tg) < 0) __builtin_amdgcn_s_sleep(4);
    tg += d;
  };
  v4u f0 = a[0];
  TNOW(t13);
  asm volatile("" : "+v"(f0));
  __builtin_amdgcn_sched_barrier(0);
  TNOW(t14);
  {
    v2f xv[4];
#pragma unroll
    for (int e = 0; e < 4; ++e) xv[e] = unpk(f0[e]);
    const v2f s2 = (xv[0] + xv[1]) + (xv[2] + xv[3]);
    const float mean = wave_sum(s2[0] + s2[1]) * (1.0f / D_);
    float ss = 0;
#pragma unroll
    for (int e = 0; e < 4; ++e) { const v2f d = xv[e] - mean; ss += d[0] * d[0] + d[1] * d[1]; }
    ss = lane < 56 ? ss : 0.f;
    const float mb = rbf(mean);
    const float rs = __builtin_amdgcn_rcpf(rbf(__builtin_amdgcn_sqrtf(rbf(rbf(wave_sum(ss) * (1.0f / D_)) + 1e-5f))));
    v4u xb;
#pragma unroll
    for (int e = 0; e < 4; ++e) { const v2f d = unpk(pkbf(xv[e][0] - mb, xv[e][1] - mb)) * rs; xb[e] = pkbf(d[0], d[1]); }
    if (lane < 56) {
      *(v4u*)(L.xa + hw * XROW + lane * 16) = xb;
      if (hw == b) {
        v4f* p = (v4f*)&L.xn[lane * 8];
        const v2f c0 = unpk(xb[0]), c1 = unpk(xb[1]), c2 = unpk(xb[2]), c3 = unpk(xb[3]);
        p[0] = v4f{c0[0], c0[1], c1[0], c1[1]};
        p[1] = v4f{c2[0], c2[1], c3[0], c3[1]};
      }
    }
    TNOW(t15);
    *(v4u*)(L.wqs + hw * (56 * 32) + lane * 16) = a[1];
    if (lane < 48) *(v4u*)(L.wqs + hw * (56 * 32) + 1024 + lane * 16) = a[2];
    asm volatile("s_waitcnt lgkmcnt(0)" ::: "memory");
    *(lds_u*)&L.ml[w][0] = 1u;
  }
  TNOW(t9);
  const unsigned tlo = ep[0] + 0x01010101u, thi = ep[1] + 0x01010101u;
  if (hw == 7) {
#pragma unroll 1
    for (unsigned spin = 0; spin < SPIN_MAX; ++spin)
      if (__builtin_amdgcn_ballot_w64(*(lds_u*)&L.ml[4 + (lane & 7)][0] == 0u) == 0) break;
    asm volatile("" ::: "memory");
    TNOW(t10);
    const int kg = lane >> 4, nn = lane & 15;
    typedef __attribute__((address_space(3))) v4s lds_v4s;
    const char* ap = L.xa + (nn & 7) * XROW + kg * 16;
    const char* bp = L.wqs + kg * 256 + nn * 8;
    v4f d0 = {0, 0, 0, 0}, d1 = {0, 0, 0, 0};
#pragma unroll
    for (int ks = 0; ks < 14; ++ks) {
      const v8bf af = *(const v8bf*)(ap + ks * 64);
      const v4s b0 = __builtin_amdgcn_ds_read_tr16_b64_v4i16((lds_v4s*)(bp + ks * 1024));
      const v4s b1 = __builtin_amdgcn_ds_read_tr16_b64_v4i16((lds_v4s*)(bp + ks * 1024 + 128));
      const v8bf bfr = __builtin_bit_cast(v8bf, __builtin_shufflevector(b0, b1, 0, 1, 2, 3, 4, 5, 6, 7));
      if (ks & 1) d1 = __builtin_amdgcn_mfma_f32_16x16x32_bf16(af, bfr, d1, 0, 0, 0);
      else d0 = __builtin_amdgcn_mfma_f32_16x16x32_bf16(af, bfr, d0, 0, 0, 0);
    }
    d0 += d1;
    const bool odd = nn & 1;
    const float u0 = kg < 2 ? d0[0] : d0[2], u1 = kg < 2 ? d0[1] : d0[3];
    const float keep = odd ? u1 : u0, recv = dpp<DPP_X1>(odd ? u0 : u1);
    const unsigned vs = WS_Q + qg * 512 + ((kg & 1) * 4 + (kg >> 1) * 2 + (nn & 1)) * 64 + (nn >> 1) * 8;
    __builtin_amdgcn_raw_buffer_store_b64(v2u{pkbf(odd ? recv : keep, odd ? keep : recv), (nn & 2) ? thi : tlo}, rws, vs, 0, SC1);
    TNOW(t11);
    *(v2u*)L.tag = v2u{tlo, thi};
    asm volatile("s_waitcnt vmcnt(0)" ::: "memory");
    TNOW(t5);
    v4u g;
#pragma unroll 1
    for (unsigned spin = 0; spin < SPIN_MAX; ++spin) {
      asm volatile("" ::: "memory");
      g = __builtin_amdgcn_raw_buffer_load_b128(rws, WS_Q + (h * 16 + (lane >> 2)) * 512 + b * 64 + (lane & 3) * 16, 0, SC1);
      if (__builtin_amdgcn_ballot_w64(g[1] != tlo || g[3] != thi) == 0) break;
    }
    *(v2u*)&L.q[lane * 2] = v2u{g[0], g[2]};
    STAMP(12); STAMPV(11, t11); STAMPV(10, t10); STAMPV(5, t5);
  }
  wg_barrier();
  if (hw == SH) { STAMPV(9, t9); STAMPV(13, t13); STAMPV(14, t14); STAMPV(15, t15_); }

  if (hw < 7) {
    const int kg = lane >> 4, nn = lane & 15;
    const rsrc_t ro = mk(wo + (size_t)h * (DH_ * D_ * 2), DH_ * D_ * 2);
    const int vo = kg * 8 * (D_ * 2) + hw * 128 + nn * 8;
    v4u bf[8][4];
    const unsigned ytag = L.tag[nn & 1], gtag = L.tag[0];
    const bool gov = (GOV >> hw) & 1, agg = bx == 61 && ((AGG >> hw) & 1);
#pragma unroll
    for (int ks = 0; ks < 8; ++ks) {
      v2u t[8];
      gate(WOD);
      v2u gv;
      if (ks) {
        if (b == 3 && ((PUB >> hw) & 1)) {
          int d = (int)(*(lds_u*)&L.lag[lane & 3] - t0);
          d += __builtin_amdgcn_update_dpp(0, d, DPP_X1, 0xf, 0xf, true);
          d += __builtin_amdgcn_update_dpp(0, d, DPP_X2, 0xf, 0xf, true);
          __builtin_amdgcn_raw_buffer_store_b64(v2u{t0 + (unsigned)(d >> 2), gtag}, rws, lane == 0 ? WS_P + gs * 512 : OOB, 0, SC1);
        }
        gv = __builtin_amdgcn_raw_buffer_load_b64(rws, agg ? WS_P + l31 * 512 : gov ? WS_G + (bx & 63) * RST : OOB, 0, SC1);
      }
#pragma unroll
      for (int e = 0; e < 8; ++e) t[e] = __builtin_amdgcn_raw_buffer_load_b64(ro, vo, (ks * 32 + e) * (D_ * 2), NT);
#pragma unroll
      for (int d = 0; d < 4; ++d) {
        bf[ks][0][d] = __builtin_amdgcn_perm(t[2 * d + 1][0], t[2 * d][0], 0x05040100u);
        bf[ks][1][d] = __builtin_amdgcn_perm(t[2 * d + 1][0], t[2 * d][0], 0x07060302u);
        bf[ks][2][d] = __builtin_amdgcn_perm(t[2 * d + 1][1], t[2 * d][1], 0x05040100u);
        bf[ks][3][d] = __builtin_amdgcn_perm(t[2 * d + 1][1], t[2 * d][1], 0x07060302u);
      }
      asm volatile("" : "+v"(bf[ks][0]), "+v"(bf[ks][1]), "+v"(bf[ks][2]), "+v"(bf[ks][3]));
      __builtin_amdgcn_sched_barrier(0);
      if (ks && (gov || agg) && __builtin_amdgcn_ballot_w64(gv[1] != gtag) == 0) {
        if (agg) {
          const float d = wave_sum((float)min(max((int)(gv[0] - t0), -2000), 2000));
          __builtin_amdgcn_raw_buffer_store_b64(v2u{t0 + (unsigned)((int)(d * (1.0f / 64)) + DELTA), gtag}, rws, WS_G + lane * RST, 0, SC1);
        } else *(lds_u*)&L.gate = gv[0];
      }
    }
    wg_barrier();
    const char* ap = L.a + nn * AROW + kg * 16;
    v4f dd[4] = {{0, 0, 0, 0}, {0, 0, 0, 0}, {0, 0, 0, 0}, {0, 0, 0, 0}};
    const v4f m01 = *(const v4f*)&L.ml[0][0], m23 = *(const v4f*)&L.ml[2][0];
    const float mk_ = L.ml[kg][0], wgt = L.wgt;
    const v4f zz = *(const v4f*)&L.z[hw * 64 + nn * 4];
#pragma unroll
    for (int ks = 0; ks < 8; ++ks) {
      const v8bf af = *(const v8bf*)(ap + ks * 64);
#pragma unroll
      for (int t = 0; t < 4; ++t)
        dd[t] = __builtin_amdgcn_mfma_f32_16x16x32_bf16(af, __builtin_bit_cast(v8bf, bf[ks][t]), dd[t], 0, 0, 0);
    }
    const float sc = ex2(mk_ - fmaxf(fmaxf(m01[0], m01[2]), fmaxf(m23[0], m23[2]))) * wgt;
    v4u yb;
#pragma unroll
    for (int t = 0; t < 4; ++t) {
      float y = ((dd[t][0] + dd[t][1]) + (dd[t][2] + dd[t][3])) * sc;
      y = mrg16(y, y);
      y = mrg32(y, y) + zz[t];
      yb[t] = (asu(y) & 0xffffff00u) | ((ytag >> (8 * t)) & 0xffu);
    }
    __builtin_amdgcn_raw_buffer_store_b128(yb, rws, lane < 16 ? WS_Y + bid * SLOT + hw * 256 + nn * 16 : OOB, 0, SC1);
    if (hw == 0) STAMP(4);
    return;
  }

  v2f z[4] = {{0, 0}, {0, 0}, {0, 0}, {0, 0}};
  if (gs < 28) {
    const int rg = lane >> 3;
    const rsrc_t rgt = mk(wg, D_ * F_ * 2), rup = mk(wu, D_ * F_ * 2);
    const int vo = rg * 56 * (F_ * 2) + gs * 128 + (lane & 7) * 16;
    v2f g[4] = {{0, 0}, {0, 0}, {0, 0}, {0, 0}}, u[4] = {{0, 0}, {0, 0}, {0, 0}, {0, 0}};
    v4u ta[4], tb[4];
    auto ldgu = [&](v4u* t, int c) __attribute__((always_inline)) {
      gate(GLD);
#pragma unroll
      for (int i = 0; i < 2; ++i) {
        t[i] = __builtin_amdgcn_raw_buffer_load_b128(rgt, vo, (c * 2 + i) * (F_ * 2), NT);
        t[i + 2] = __builtin_amdgcn_raw_buffer_load_b128(rup, vo, (c * 2 + i) * (F_ * 2), NT);
      }
      __builtin_amdgcn_sched_barrier(0);
    };
    auto usegu = [&](const v4u* t, float x0, float x1) __attribute__((always_inline)) {
#pragma unroll
      for (int e = 0; e < 4; ++e) {
        g[e] = __builtin_elementwise_fma(unpk(t[0][e]), v2f{x0, x0}, g[e]);
        g[e] = __builtin_elementwise_fma(unpk(t[1][e]), v2f{x1, x1}, g[e]);
        u[e] = __builtin_elementwise_fma(unpk(t[2][e]), v2f{x0, x0}, u[e]);
        u[e] = __builtin_elementwise_fma(unpk(t[3][e]), v2f{x1, x1}, u[e]);
      }
      asm volatile("" : "+v"(g[0]), "+v"(g[1]), "+v"(g[2]), "+v"(g[3]), "+v"(u[0]), "+v"(u[1]), "+v"(u[2]), "+v"(u[3]));
      __builtin_amdgcn_sched_barrier(0);
    };
    ldgu(ta, 0);
#pragma unroll 1
    for (int c = 0; c < 28; c += 2) {
      const v4f xx = *(const v4f*)&L.xn[rg * 56 + c * 2];
      ldgu(tb, c + 1);
      usegu(ta, xx[0], xx[1]);
      ldgu(ta, min(c + 2, 27));
      usegu(tb, xx[2], xx[3]);
    }
    const rsrc_t rd = mk(wd + gs * (64 * D_ * 2), 64 * D_ * 2);
    const unsigned vd = lane < 56 ? (unsigned)lane * 16u : OOB;
    auto ldd = [&](v4u* t, int c) __attribute__((always_inline)) {
      gate(GLD);
#pragma unroll
      for (int i = 0; i < 4; ++i) t[i] = __builtin_amdgcn_raw_buffer_load_b128(rd, vd, (c * 4 + i) * (D_ * 2), NT);
      __builtin_amdgcn_sched_barrier(0);
    };
    ldd(ta, 0);
    float hv[8];
#pragma unroll
    for (int e = 0; e < 8; ++e) {
      float gg = g[e >> 1][e & 1], uu = u[e >> 1][e & 1];
      gg += dpp<DPP_R8>(gg); gg = mrg16(gg, gg); gg = mrg32(gg, gg);
      uu += dpp<DPP_R8>(uu); uu = mrg16(uu, uu); uu = mrg32(uu, uu);
      hv[e] = rbf(rbf(1.0f / (1.0f + expf(-rbf(gg)))) * rbf(uu));
    }
    if (lane < 8) {
      *(v4f*)&L.hv[lane * 8] = v4f{hv[0], hv[1], hv[2], hv[3]};
      *(v4f*)&L.hv[lane * 8 + 4] = v4f{hv[4], hv[5], hv[6], hv[7]};
    }
    auto used = [&](const v4u* t, const v4f hh) __attribute__((always_inline)) {
#pragma unroll
      for (int k = 0; k < 4; ++k)
#pragma unroll
        for (int e = 0; e < 4; ++e) z[e] = __builtin_elementwise_fma(unpk(t[k][e]), v2f{hh[k], hh[k]}, z[e]);
      asm volatile("" : "+v"(z[0]), "+v"(z[1]), "+v"(z[2]), "+v"(z[3]));
      __builtin_amdgcn_sched_barrier(0);
    };
#pragma unroll 1
    for (int c = 0; c < 16; c += 2) {
      const v4f h0 = *(const v4f*)&L.hv[c * 4], h1 = *(const v4f*)&L.hv[c * 4 + 4];
      ldd(tb, c + 1);
      used(ta, h0);
      ldd(ta, min(c + 2, 15));
      used(tb, h1);
    }
  }
  if (lane < 56) {
    *(v4f*)&L.z[lane * 8] = v4f{z[0][0], z[0][1], z[1][0], z[1][1]};
    *(v4f*)&L.z[lane * 8 + 4] = v4f{z[2][0], z[2][1], z[3][0], z[3][1]};
  }
  {
    v2f ml;
#pragma unroll 1
    for (unsigned spin = 0; spin < SPIN_MAX; ++spin) {
      ml = *(lds_v2f*)&L.ml[lane & 3][0];
      if (__builtin_amdgcn_ballot_w64(asu(ml[1]) == 0xBF800000u) == 0) break;
      __builtin_amdgcn_s_sleep(16);
    }
    float m = fmaxf(ml[0], dpp<DPP_X1>(ml[0]));
    m = fmaxf(m, dpp<DPP_X2>(m));
    float l = ml[1] * ex2(ml[0] - m);
    l += dpp<DPP_X1>(l);
    l += dpp<DPP_X2>(l);
    const v2u gr = (lane & 1) ? v2u{asu(l), thi} : v2u{asu(m), tlo};
    __builtin_amdgcn_raw_buffer_store_b64(gr, rws, lane < 2 ? WS_REC + bid * 128 + lane * 8 : OOB, 0, SC1);
    v4u g;
#pragma unroll 1
    for (unsigned spin = 0; spin < SPIN_MAX; ++spin) {
      asm volatile("" ::: "memory");
      g = __builtin_amdgcn_raw_buffer_load_b128(rws, WS_REC + (bid ^ 1) * 128, 0, SC1);
      if (__builtin_amdgcn_ballot_w64(g[1] != tlo || g[3] != thi) == 0) break;
      __builtin_amdgcn_s_sleep(64);
    }
    const float mo = asf(g[0]), M = fmaxf(m, mo), e = ex2(m - M), eo = ex2(mo - M);
    const float la = l * e, lb = asf(g[2]) * eo;
    L.wgt = e / ((j & 1) ? lb + la : la + lb);
  }
  if (j >= 28) { wg_barrier(); return; }
  const unsigned vy = (b * 32 + l31) * SLOT + j * 64 + half * 32;
  const int col = j * 16 + half * 8 + ((lane >> 4) & 1) + ((lane >> 2) & 2) + (lane & 4);
  const float xres = asf((unsigned)__builtin_amdgcn_raw_buffer_load_b16(mk(x + b * (D_ * 2), D_ * 2), col * 2, 0, 0) << 16);
  const bool b3 = lane & 8, b2 = lane & 4;
  auto red = [&](const float* v) -> float {
    float r = mrg<DPP_HM>(mrg<DPP_M>(mrg16(v[0], v[1]), mrg16(v[2], v[3]), b3), mrg<DPP_M>(mrg16(v[4], v[5]), mrg16(v[6], v[7]), b3), b2);
    r += dpp<DPP_X2>(r);
    r += dpp<DPP_X1>(r);
    return r;
  };
  STAMP(6);
  wg_barrier();
  v4u y0, y1;
#pragma unroll 1
  for (unsigned spin = 0; spin < SPIN_MAX; ++spin) {
    asm volatile("" ::: "memory");
    y0 = __builtin_amdgcn_raw_buffer_load_b128(rws, WS_Y + vy, 0, SC1);
    y1 = __builtin_amdgcn_raw_buffer_load_b128(rws, WS_Y + vy + 16, 0, SC1);
    const unsigned c0 = __builtin_amdgcn_perm(__builtin_amdgcn_perm(y0[3], y0[2], 0x0400u), __builtin_amdgcn_perm(y0[1], y0[0], 0x0400u), 0x05040100u);
    const unsigned c1 = __builtin_amdgcn_perm(__builtin_amdgcn_perm(y1[3], y1[2], 0x0400u), __builtin_amdgcn_perm(y1[1], y1[0], 0x0400u), 0x05040100u);
    if (__builtin_amdgcn_ballot_w64(c0 != tlo || c1 != thi) == 0) break;
    __builtin_amdgcn_s_sleep(2);
  }
  float v[8];
#pragma unroll
  for (int e = 0; e < 4; ++e) { v[e] = asf(y0[e] & 0xffffff00u); v[e + 4] = asf(y1[e] & 0xffffff00u); }
  const unsigned ob = pkbf(xres + red(v), 0.f);
  __builtin_amdgcn_raw_buffer_store_b16((unsigned short)ob, mk(out + b * (D_ * 2), D_ * 2), (lane & 3) == 0 ? (unsigned)col * 2u : OOB, 0, 0);
  if (bid == 0) __builtin_amdgcn_raw_buffer_store_b64(v2u{tlo, thi}, rws, lane == 0 ? WS_E : OOB, 0, SC1);
  STAMP(7);
}

extern "C" void kernel_launch(void* const* d_in, const int* in_sizes, int n_in, void* d_out, int out_size,
                              void* d_ws, size_t ws_size, hipStream_t stream) {
  (void)in_sizes; (void)n_in; (void)out_size; (void)ws_size;
  fused_decode_kernel<<<256, 768, 0, stream>>>((const char*)d_in[0], (const char*)d_in[1], (const char*)d_in[3], (char*)d_ws,
                                               (const char*)d_in[2], (const char*)d_in[4], (const char*)d_in[6],
                                               (const char*)d_in[7], (const char*)d_in[8], (char*)d_out);
}
```

```cpp
#include <hip/hip_runtime.h>
#include <stdint.h>

typedef unsigned v4u __attribute__((ext_vector_type(4)));
typedef unsigned v2u __attribute__((ext_vector_type(2)));
typedef float v4f __attribute__((ext_vector_type(4)));
typedef float v2f __attribute__((ext_vector_type(2)));
typedef __bf16 v8bf __attribute__((ext_vector_type(8)));
typedef __bf16 v2bf __attribute__((ext_vector_type(2)));
typedef __amdgpu_buffer_rsrc_t rsrc_t;
typedef __attribute__((address_space(3))) volatile v2f lds_v2f;
typedef __attribute__((address_space(3))) volatile unsigned lds_u;
typedef short v4s __attribute__((ext_vector_type(4)));
typedef short v8s __attribute__((ext_vector_type(8)));

namespace {

constexpr int D_ = 448, DH_ = 256, F_ = 1792;
constexpr unsigned WS_E = 0, WS_P = 4096, WS_REC = 32768, WS_Q = 65536, WS_Y = 256u << 10, WS_G = 1u << 20, WS_SPAN = 2u << 20;
constexpr unsigned SLOT = D_ * 4;
constexpr unsigned OOB = 0x80000000u;
constexpr unsigned SPIN_MAX = 1u << 22;
constexpr int NX = 8;
constexpr int PW = 8;
constexpr int WX = 10, LIM = 7, LIM2 = 5;
constexpr int PN = 116, SMX = 30;
constexpr int DELTA = 35, BAND = 50, EXT = 9;
constexpr int WO0 = 800, WOS = 100, WOD = 600;
constexpr int GL0 = 750, GLD = 110;
constexpr unsigned long long PERM = 0x63a987125b40ull;
constexpr int NT = 2, SC1 = 16;
constexpr int AROW = 528;
constexpr int XROW = 912;

struct __attribute__((aligned(16))) Lds {
  float xn[D_];
  char xa[8 * XROW];
  char wqs[D_ * 32];
  float z[D_];
  unsigned q[DH_ / 2];
  unsigned tag[2];
  float wgt;
  unsigned gate;
  unsigned lag[4];
  float p[4][8][2][32];
  float hv[64];
  float ml[12][2];
  char a[16 * AROW];
  v4u kx[4][NX][64];
  unsigned epd;
};

__device__ __forceinline__ rsrc_t mk(const void* p, unsigned n) {
  return __builtin_amdgcn_make_buffer_rsrc((void*)p, 0, n, 0x00020000);
}
__device__ __forceinline__ float asf(unsigned u) { return __builtin_bit_cast(float, u); }
__device__ __forceinline__ unsigned asu(float f) { return __builtin_bit_cast(unsigned, f); }
__device__ __forceinline__ v2f unpk(unsigned d) { return v2f{asf(d << 16), asf(d & 0xffff0000u)}; }
template <int C> __device__ __forceinline__ float dpp(float v) {
  return asf(__builtin_amdgcn_update_dpp(0, asu(v), C, 0xf, 0xf, true));
}
constexpr int DPP_X1 = 0xB1, DPP_X2 = 0x4E, DPP_HM = 0x141, DPP_M = 0x140, DPP_R8 = 0x128, DPP_R4 = 0x124;
__device__ __forceinline__ float mrg16(float a, float b) {
  auto r = __builtin_amdgcn_permlane16_swap(asu(a), asu(b), false, false);
  return asf(r[0]) + asf(r[1]);
}
__device__ __forceinline__ float mrg32(float a, float b) {
  auto r = __builtin_amdgcn_permlane32_swap(asu(a), asu(b), false, false);
  return asf(r[0]) + asf(r[1]);
}
template <int C> __device__ __forceinline__ float mrg(float a, float b, bool hi) {
  const float keep = hi ? b : a, send = hi ? a : b;
  return keep + dpp<C>(send);
}
__device__ __forceinline__ float wave_sum(float v) {
  v += dpp<DPP_X1>(v); v += dpp<DPP_X2>(v); v += dpp<DPP_HM>(v); v += dpp<DPP_M>(v);
  v = mrg16(v, v); v = mrg32(v, v);
  return v;
}
__device__ __forceinline__ float wave_max(float v) {
  v = fmaxf(v, dpp<DPP_X1>(v)); v = fmaxf(v, dpp<DPP_X2>(v));
  v = fmaxf(v, dpp<DPP_HM>(v)); v = fmaxf(v, dpp<DPP_M>(v));
  auto r = __builtin_amdgcn_permlane16_swap(asu(v), asu(v), false, false);
  v = fmaxf(asf(r[0]), asf(r[1]));
  r = __builtin_amdgcn_permlane32_swap(asu(v), asu(v), false, false);
  return fmaxf(asf(r[0]), asf(r[1]));
}
__device__ __forceinline__ unsigned pkbf(float a, float b) {
  return __builtin_bit_cast(unsigned, __builtin_convertvector(v2f{a, b}, v2bf));
}
__device__ __forceinline__ float rbf(float a) { return asf(pkbf(a, 0.f) << 16); }
__device__ __forceinline__ float dot2(unsigned a, unsigned b, float c) {
  return __builtin_amdgcn_fdot2_f32_bf16(__builtin_bit_cast(v2bf, a), __builtin_bit_cast(v2bf, b), c, false);
}
__device__ __forceinline__ void wg_barrier() { asm volatile("s_waitcnt lgkmcnt(0)\n\ts_barrier" ::: "memory"); }
__device__ __forceinline__ float ex2(float x) { return __builtin_amdgcn_exp2f(x); }

}

__global__ void __launch_bounds__(768, 3)
fused_decode_kernel(const char* __restrict__ x, const char* __restrict__ kc, const char* __restrict__ wq, char* ws,
                    const char* __restrict__ vc, const char* __restrict__ wo, const char* __restrict__ wg,
                    const char* __restrict__ wu, const char* __restrict__ wd, char* out) {
  __shared__ Lds L;
  const unsigned t0 = (unsigned)__builtin_amdgcn_s_memrealtime();
  __builtin_amdgcn_sched_barrier(0);
  asm volatile("" ::"s"(x), "s"(kc), "s"(wq), "s"(ws));
  const int lane = threadIdx.x & 63;
  const int w = 11 - __builtin_amdgcn_readfirstlane(threadIdx.x >> 6);
  const int bx = blockIdx.x, b = bx >> 5, gs = bx & 31;
  const int j = (bx & 7) * 2 + ((bx >> 3) & 1) + (bx & 16);
  const int h = j >> 1, bid = b * 32 + j;
  const int half = lane >> 5, l31 = lane & 31;
  const int hw = w - 4;
  const char* kb = kc + (size_t)(bid >> 1) * (2u << 20);
  auto pb = [&](int k) -> unsigned { return 512u << ((unsigned)(PERM >> (4 * k)) & 15u); };
  const int voff = l31 * 16 + (half ? pb(0) : 0);
  const unsigned rot = ((w & 1) ? pb(1) : 0) ^ ((w & 2) ? pb(2) : 0) ^ ((bid & 1) ? pb(11) : 0);
  auto so = [&](int i) -> unsigned {
    unsigned v = 0;
#pragma unroll
    for (int k = 0; k < 8; ++k) v |= ((i >> k) & 1) ? pb(3 + k) : 0u;
    return v;
  };
  const int qg = j * 8 + b;
  const rsrc_t rws = mk(ws, WS_SPAN);
  v4u a[32];
  v2u ep;
  {
    if (w < 4) {
      const rsrc_t rk = mk(kb, 2u << 20);
#pragma unroll
      for (int i = 0; i < PW; ++i) a[i] = __builtin_amdgcn_raw_buffer_load_b128(rk, voff, rot ^ so(i), NT);
    } else {
      const rsrc_t r1 = mk(wq + qg * 32, 447 * 8192 + 32);
      const int vq = (lane >> 1) * 8192 + (lane & 1) * 16;
      a[0] = __builtin_amdgcn_raw_buffer_load_b128(mk(x + hw * (D_ * 2), D_ * 2), lane * 16, 0, 0);
      a[1] = __builtin_amdgcn_raw_buffer_load_b128(r1, vq, hw * (56 * 8192), NT);
      a[2] = __builtin_amdgcn_raw_buffer_load_b128(r1, lane < 48 ? vq : (int)OOB, hw * (56 * 8192) + 32 * 8192, NT);
      ep = __builtin_amdgcn_raw_buffer_load_b64(rws, hw == 7 ? WS_E : OOB, 0, SC1);
    }
    *(lds_v2f*)&L.ml[w][0] = v2f{0.f, -1.f};
    *(lds_u*)&L.gate = t0 - 1000000u;
    *(lds_u*)&L.lag[lane & 3] = t0 + 300;
    *(lds_u*)&L.epd = 0;
    wg_barrier();
    __builtin_amdgcn_sched_barrier(0);
  }

  if (w < 4) {
    const char* vb = vc + (size_t)(bid >> 1) * (2u << 20);
    __builtin_amdgcn_s_setprio(3);
    {
      const rsrc_t rk = mk(kb, 2u << 20);
#pragma unroll
      for (int i = PW; i < 32; ++i) {
        asm volatile("s_waitcnt vmcnt(%0)" ::"n"(PW - 1) : "memory");
        a[i] = __builtin_amdgcn_raw_buffer_load_b128(rk, voff, rot ^ so(i), NT);
      }
#pragma unroll
      for (int i = 0; i < NX; ++i) {
        asm volatile("s_waitcnt vmcnt(%0)" ::"n"(PW - 1) : "memory");
        __builtin_amdgcn_raw_ptr_buffer_load_lds(rk, (__attribute__((address_space(3))) void*)&L.kx[w][i][0], 16, voff, rot ^ so(32 + i), 0, NT);
      }
    }
    __builtin_amdgcn_sched_barrier(0);
    wg_barrier();
    v4u q;
    asm volatile("ds_read_b128 %0, %1\n\ts_waitcnt lgkmcnt(0)" : "=v"(q) : "v"((unsigned)(size_t)(lds_u*)&L.q[l31 * 4]) : "memory");
    const bool b3 = lane & 8, b2 = lane & 4, b1 = lane & 2, b0 = lane & 1;
    const int rev = __builtin_bitreverse32(l31) >> 27;
    float* pw = &L.p[w][0][half][rev];
    float mx = -__builtin_inff();
    unsigned gnext = t0 - 1000000u, kk = 7 * PN;
    int deep = 1;
    auto pace = [&](const bool force) __attribute__((always_inline)) {
      const unsigned lag = (unsigned)__builtin_amdgcn_s_memrealtime() - kk;
      deep = force ? 1 : (int)(lag - __builtin_amdgcn_readfirstlane(gnext));
      *(lds_u*)&L.lag[w] = lag;
      gnext = *(lds_u*)&L.gate;
      kk += PN;
    };
    auto limit = [&]() __attribute__((always_inline)) {
      asm volatile("s_cmp_gt_i32 %0, 0\n\ts_cbranch_scc1 1f\n\ts_waitcnt vmcnt(%1)\n\ts_cmp_gt_i32 %0, %3\n\ts_cbranch_scc1 1f\n\ts_waitcnt vmcnt(%2)\n1:"
                   ::"s"(deep), "n"(LIM - 1), "n"(LIM2 - 1), "s"(-BAND) : "scc");
    };
    auto kround = [&](const int r, const int mode) __attribute__((always_inline)) {
      const rsrc_t rc = mk(kb, 2u << 20), rn = mk(r < 7 ? kb : vb, 2u << 20);
      const unsigned sc = rot ^ so(r * 32), sn = rot ^ so(((r + 1) & 7) * 32);
      const int sh = mode == 1 ? NX : 0;
      float p0 = 0, p1 = 0, p2 = 0, p3 = 0, p4 = 0;
      if (mode == 1) {
        asm volatile("s_waitcnt vmcnt(%0)" ::"n"(WX) : "memory");
#pragma unroll
        for (int i = 0; i < NX; ++i) a[32 - NX + i] = L.kx[w][i][lane];
      }
#pragma unroll
      for (int i = 0; i < 32; ++i) {
        const v4u c = a[(i - sh) & 31];
        float v = dot2(c[0], q[0], 0.f), v2 = dot2(c[1], q[1], 0.f);
        v = dot2(c[2], q[2], v); v2 = dot2(c[3], q[3], v2);
        v += v2;
        asm volatile("" : "+v"(v));
        if (mode == 0) {
          if (i < PW) a[i] = __builtin_amdgcn_raw_buffer_load_b128(rn, voff, sn ^ so(NX + i), NT);
          if (i >= 32 - (WX - PW)) a[i - 32 + WX] = __builtin_amdgcn_raw_buffer_load_b128(rn, voff, sn ^ so(NX + i - 32 + WX), NT);
        } else if (i >= sh) {
          if (mode == 2 && (i & 7) == 0) pace(false);
          limit();
          if (i + WX >= 32) a[(i + WX) & 31] = __builtin_amdgcn_raw_buffer_load_b128(rn, voff, sn ^ so((i + WX) & 31), NT);
          else a[i + WX - sh] = __builtin_amdgcn_raw_buffer_load_b128(rc, voff, sc ^ so(i + WX), NT);
        }
        if (i & 1) { v = mrg16(p0, v);
          if (i & 2) { v = mrg<DPP_M>(p1, v, b3);
            if (i & 4) { v = mrg<DPP_HM>(p2, v, b2);
              if (i & 8) { v = mrg<DPP_X2>(p3, v, b1);
                if (i & 16) { v = rbf(mrg<DPP_X1>(p4, v, b0)) * (0.0625f * 1.44269504088896340736f); pw[r * 64] = v; mx = fmaxf(mx, v); }
                else p4 = v; }
              else p3 = v; }
            else p2 = v; }
          else p1 = v; }
        else p0 = v;
        __builtin_amdgcn_sched_barrier(0);
      }
    };
    kround(0, 0);
    kround(1, 1);
#pragma unroll 1
    for (int r = 2; r < 8; ++r) kround(r, 2);
    {
      mx = wave_max(mx);
      float sum = 0;
#pragma unroll
      for (int r = 0; r < 8; ++r) { const float p = ex2(pw[r * 64] - mx); pw[r * 64] = p; sum += p; }
      sum = wave_sum(sum);
      if (lane == 0) *(lds_v2f*)&L.ml[w][0] = v2f{mx, sum};
    }
    kk += SMX;
    v2f acc[4] = {{0, 0}, {0, 0}, {0, 0}, {0, 0}};
    const v4f* pr = (const v4f*)&L.p[w][0][half][0];
#pragma unroll 1
    for (int r = 0; r < 8; ++r) {
      const rsrc_t rc = mk(vb, 2u << 20), rn = mk(vb, r < 7 ? 2u << 20 : 0u);
      const unsigned sc = rot ^ so(r * 32), sn = rot ^ so(((r + 1) & 7) * 32);
      v4f pv = pr[r * 16];
#pragma unroll
      for (int i = 0; i < 32; ++i) {
        auto refill = [&]() __attribute__((always_inline)) {
          if ((i & 7) == 0) pace(i >= 16 && r == 7);
          limit();
          if (i + WX >= 32) a[(i + WX) & 31] = __builtin_amdgcn_raw_buffer_load_b128(rn, voff, sn ^ so((i + WX) & 31), NT);
          else a[i + WX] = __builtin_amdgcn_raw_buffer_load_b128(rc, voff, sc ^ so(i + WX), NT);
        };
        const float p = pv[i & 3];
        if ((i & 3) == 3 && i < 31) pv = pr[r * 16 + (i + 1) / 4];
        const v2f pp = {p, p};
#pragma unroll
        for (int e = 0; e < 4; ++e) acc[e] = __builtin_elementwise_fma(unpk(a[i][e]), pp, acc[e]);
        asm volatile("" : "+v"(acc[0]), "+v"(acc[1]), "+v"(acc[2]), "+v"(acc[3]));
        refill();
        __builtin_amdgcn_sched_barrier(0);
      }
    }
    {
      v4u hi, lo;
#pragma unroll
      for (int e = 0; e < 4; ++e) {
        hi[e] = pkbf(acc[e][0], acc[e][1]);
        const v2f r = acc[e] - unpk(hi[e]);
        lo[e] = pkbf(r[0], r[1]);
      }
      char* ap = L.a + (w * 4 + half * 2) * AROW + l31 * 16;
      *(v4u*)ap = hi;
      *(v4u*)(ap + AROW) = lo;
    }
    wg_barrier();
    asm volatile("s_endpgm" : "=v"(a[0]), "=v"(a[1]), "=v"(a[2]), "=v"(ep)::"memory");
  }

  unsigned tg = t0 + (hw < 7 ? WO0 + hw * WOS : GL0);
  auto gate = [&](unsigned d) __attribute__((always_inline)) {
    while ((int)((unsigned)__builtin_amdgcn_s_memrealtime() - tg) < 0) __builtin_amdgcn_s_sleep(4);
    tg += d;
  };
  v4u f0 = a[0];
  asm volatile("" : "+v"(f0));
  __builtin_amdgcn_sched_barrier(0);
  {
    v2f xv[4];
#pragma unroll
    for (int e = 0; e < 4; ++e) xv[e] = unpk(f0[e]);
    const v2f s2 = (xv[0] + xv[1]) + (xv[2] + xv[3]);
    const float mean = wave_sum(s2[0] + s2[1]) * (1.0f / D_);
    float ss = 0;
#pragma unroll
    for (int e = 0; e < 4; ++e) { const v2f d = xv[e] - mean; ss += d[0] * d[0] + d[1] * d[1]; }
    ss = lane < 56 ? ss : 0.f;
    const float mb = rbf(mean);
    const float rs = __builtin_amdgcn_rcpf(rbf(__builtin_amdgcn_sqrtf(rbf(rbf(wave_sum(ss) * (1.0f / D_)) + 1e-5f))));
    v4u xb;
#pragma unroll
    for (int e = 0; e < 4; ++e) { const v2f d = unpk(pkbf(xv[e][0] - mb, xv[e][1] - mb)) * rs; xb[e] = pkbf(d[0], d[1]); }
    if (lane < 56) {
      *(v4u*)(L.xa + hw * XROW + lane * 16) = xb;
      if (hw == b) {
        v4f* p = (v4f*)&L.xn[lane * 8];
        const v2f c0 = unpk(xb[0]), c1 = unpk(xb[1]), c2 = unpk(xb[2]), c3 = unpk(xb[3]);
        p[0] = v4f{c0[0], c0[1], c1[0], c1[1]};
        p[1] = v4f{c2[0], c2[1], c3[0], c3[1]};
      }
    }
    *(v4u*)(L.wqs + hw * (56 * 32) + lane * 16) = a[1];
    if (lane < 48) *(v4u*)(L.wqs + hw * (56 * 32) + 1024 + lane * 16) = a[2];
    asm volatile("s_waitcnt lgkmcnt(0)" ::: "memory");
    *(lds_u*)&L.ml[w][0] = 1u;
  }
  const unsigned tlo = ep[0] + 0x01010101u, thi = ep[1] + 0x01010101u;
  if (hw == 7) {
#pragma unroll 1
    for (unsigned spin = 0; spin < SPIN_MAX; ++spin)
      if (__builtin_amdgcn_ballot_w64(*(lds_u*)&L.ml[4 + (lane & 7)][0] == 0u) == 0) break;
    asm volatile("" ::: "memory");
    const int kg = lane >> 4, nn = lane & 15;
    typedef __attribute__((address_space(3))) v4s lds_v4s;
    const char* ap = L.xa + (nn & 7) * XROW + kg * 16;
    const char* bp = L.wqs + kg * 256 + nn * 8;
    v4f d0 = {0, 0, 0, 0}, d1 = {0, 0, 0, 0};
#pragma unroll
    for (int ks = 0; ks < 14; ++ks) {
      const v8bf af = *(const v8bf*)(ap + ks * 64);
      const v4s b0 = __builtin_amdgcn_ds_read_tr16_b64_v4i16((lds_v4s*)(bp + ks * 1024));
      const v4s b1 = __builtin_amdgcn_ds_read_tr16_b64_v4i16((lds_v4s*)(bp + ks * 1024 + 128));
      const v8bf bfr = __builtin_bit_cast(v8bf, __builtin_shufflevector(b0, b1, 0, 1, 2, 3, 4, 5, 6, 7));
      if (ks & 1) d1 = __builtin_amdgcn_mfma_f32_16x16x32_bf16(af, bfr, d1, 0, 0, 0);
      else d0 = __builtin_amdgcn_mfma_f32_16x16x32_bf16(af, bfr, d0, 0, 0, 0);
    }
    d0 += d1;
    const bool odd = nn & 1;
    const float u0 = kg < 2 ? d0[0] : d0[2], u1 = kg < 2 ? d0[1] : d0[3];
    const float keep = odd ? u1 : u0, recv = dpp<DPP_X1>(odd ? u0 : u1);
    const unsigned vs = WS_Q + qg * 512 + ((kg & 1) * 4 + (kg >> 1) * 2 + (nn & 1)) * 64 + (nn >> 1) * 8;
    __builtin_amdgcn_raw_buffer_store_b64(v2u{pkbf(odd ? recv : keep, odd ? keep : recv), (nn & 2) ? thi : tlo}, rws, vs, 0, SC1);
    *(v2u*)L.tag = v2u{tlo, thi};
    asm volatile("s_waitcnt vmcnt(0)" ::: "memory");
    v4u g;
#pragma unroll 1
    for (unsigned spin = 0; spin < SPIN_MAX; ++spin) {
      asm volatile("" ::: "memory");
      g = __builtin_amdgcn_raw_buffer_load_b128(rws, WS_Q + (h * 16 + (lane >> 2)) * 512 + b * 64 + (lane & 3) * 16, 0, SC1);
      if (__builtin_amdgcn_ballot_w64(g[1] != tlo || g[3] != thi) == 0) break;
    }
    *(v2u*)&L.q[lane * 2] = v2u{g[0], g[2]};
  }
  wg_barrier();

  if (hw < 7) {
    const int kg = lane >> 4, nn = lane & 15;
    const rsrc_t ro = mk(wo + (size_t)h * (DH_ * D_ * 2), DH_ * D_ * 2);
    const int vo = kg * 8 * (D_ * 2) + hw * 128 + nn * 8;
    v4u bf[8][4];
    const unsigned ytag = L.tag[nn & 1], gtag = L.tag[0];
    float dpv = 2e9f;
    const bool gov = (0x11 >> hw) & 1, agg = bx == 61 && ((0x24 >> hw) & 1);
#pragma unroll
    for (int ks = 0; ks < 8; ++ks) {
      v2u t[8];
      gate(WOD);
      v2u gv;
      if (ks) {
        if (b == 3 && ((0x09 >> hw) & 1)) {
          int d = (int)(*(lds_u*)&L.lag[lane & 3] - t0);
          d += __builtin_amdgcn_update_dpp(0, d, DPP_X1, 0xf, 0xf, true);
          d += __builtin_amdgcn_update_dpp(0, d, DPP_X2, 0xf, 0xf, true);
          __builtin_amdgcn_raw_buffer_store_b64(v2u{t0 + (unsigned)(d >> 2), gtag}, rws, lane == 0 ? WS_P + gs * 512 : OOB, 0, SC1);
        }
        gv = __builtin_amdgcn_raw_buffer_load_b64(rws, agg ? WS_P + l31 * 512 : gov ? WS_G + (bx & 63) * 512 : OOB, 0, SC1);
      }
      unsigned vt = 0;
      if (ks == 3 && hw == 1) vt = __builtin_amdgcn_raw_buffer_load_b32(mk(vc + (size_t)(bid >> 1) * (2u << 20), 2u << 20), lane < 2 ? lane * ((2u << 20) - 128) : OOB, 0, NT);
#pragma unroll
      for (int e = 0; e < 8; ++e) t[e] = __builtin_amdgcn_raw_buffer_load_b64(ro, vo, (ks * 32 + e) * (D_ * 2), NT);
#pragma unroll
      for (int d = 0; d < 4; ++d) {
        bf[ks][0][d] = __builtin_amdgcn_perm(t[2 * d + 1][0], t[2 * d][0], 0x05040100u);
        bf[ks][1][d] = __builtin_amdgcn_perm(t[2 * d + 1][0], t[2 * d][0], 0x07060302u);
        bf[ks][2][d] = __builtin_amdgcn_perm(t[2 * d + 1][1], t[2 * d][1], 0x05040100u);
        bf[ks][3][d] = __builtin_amdgcn_perm(t[2 * d + 1][1], t[2 * d][1], 0x07060302u);
      }
      asm volatile("" : "+v"(bf[ks][0]), "+v"(bf[ks][1]), "+v"(bf[ks][2]), "+v"(bf[ks][3]) : "v"(vt));
      __builtin_amdgcn_sched_barrier(0);
      if (ks && (gov || agg) && __builtin_amdgcn_ballot_w64(gv[1] != gtag) == 0) {
        if (agg) {
          if (dpv > 1e9f) {
            __builtin_amdgcn_raw_buffer_store_b32(ytag, rws, lane < 2 ? WS_E + lane * 4 : OOB, 0, SC1);
            *(lds_u*)&L.epd = 1u;
          }
          const float d = wave_sum((float)min(max((int)(gv[0] - t0), -2000), 2000)) * (1.0f / 64);
          const float ex = dpv < 1e9f ? fminf(fmaxf((d - dpv) * (EXT * 0.125f), -150.f), 150.f) : 0.f;
          dpv = d;
          __builtin_amdgcn_raw_buffer_store_b64(v2u{t0 + (unsigned)((int)(d + ex) + DELTA), gtag}, rws, WS_G + lane * 512, 0, SC1);
        } else *(lds_u*)&L.gate = gv[0];
      }
    }
    wg_barrier();
    const char* ap = L.a + nn * AROW + kg * 16;
    v4f dd[4] = {{0, 0, 0, 0}, {0, 0, 0, 0}, {0, 0, 0, 0}, {0, 0, 0, 0}};
    const v4f m01 = *(const v4f*)&L.ml[0][0], m23 = *(const v4f*)&L.ml[2][0];
    const float mk_ = L.ml[kg][0], wgt = L.wgt;
    const v4f zz = *(const v4f*)&L.z[hw * 64 + nn * 4];
#pragma unroll
    for (int ks = 0; ks < 8; ++ks) {
      const v8bf af = *(const v8bf*)(ap + ks * 64);
#pragma unroll
      for (int t = 0; t < 4; ++t)
        dd[t] = __builtin_amdgcn_mfma_f32_16x16x32_bf16(af, __builtin_bit_cast(v8bf, bf[ks][t]), dd[t], 0, 0, 0);
    }
    const float sc = ex2(mk_ - fmaxf(fmaxf(m01[0], m01[2]), fmaxf(m23[0], m23[2]))) * wgt;
    v4u yb;
#pragma unroll
    for (int t = 0; t < 4; ++t) {
      float y = ((dd[t][0] + dd[t][1]) + (dd[t][2] + dd[t][3])) * sc;
      y = mrg16(y, y);
      y = mrg32(y, y) + zz[t];
      yb[t] = (asu(y) & 0xffffff00u) | ((ytag >> (8 * t)) & 0xffu);
    }
    __builtin_amdgcn_raw_buffer_store_b128(yb, rws, lane < 16 ? WS_Y + bid * SLOT + hw * 256 + nn * 16 : OOB, 0, SC1);
    return;
  }

  v2f z[4] = {{0, 0}, {0, 0}, {0, 0}, {0, 0}};
  if (gs < 28) {
    const int rg = lane >> 3;
    const rsrc_t rgt = mk(wg, D_ * F_ * 2), rup = mk(wu, D_ * F_ * 2);
    const int vo = rg * 56 * (F_ * 2) + gs * 128 + (lane & 7) * 16;
    v2f g[4] = {{0, 0}, {0, 0}, {0, 0}, {0, 0}}, u[4] = {{0, 0}, {0, 0}, {0, 0}, {0, 0}};
    v4u ta[4], tb[4];
    auto ldgu = [&](v4u* t, int c) __attribute__((always_inline)) {
      gate(GLD);
#pragma unroll
      for (int i = 0; i < 2; ++i) {
        t[i] = __builtin_amdgcn_raw_buffer_load_b128(rgt, vo, (c * 2 + i) * (F_ * 2), NT);
        t[i + 2] = __builtin_amdgcn_raw_buffer_load_b128(rup, vo, (c * 2 + i) * (F_ * 2), NT);
      }
      __builtin_amdgcn_sched_barrier(0);
    };
    auto usegu = [&](const v4u* t, float x0, float x1) __attribute__((always_inline)) {
#pragma unroll
      for (int e = 0; e < 4; ++e) {
        g[e] = __builtin_elementwise_fma(unpk(t[0][e]), v2f{x0, x0}, g[e]);
        g[e] = __builtin_elementwise_fma(unpk(t[1][e]), v2f{x1, x1}, g[e]);
        u[e] = __builtin_elementwise_fma(unpk(t[2][e]), v2f{x0, x0}, u[e]);
        u[e] = __builtin_elementwise_fma(unpk(t[3][e]), v2f{x1, x1}, u[e]);
      }
      asm volatile("" : "+v"(g[0]), "+v"(g[1]), "+v"(g[2]), "+v"(g[3]), "+v"(u[0]), "+v"(u[1]), "+v"(u[2]), "+v"(u[3]));
      __builtin_amdgcn_sched_barrier(0);
    };
    ldgu(ta, 0);
#pragma unroll 1
    for (int c = 0; c < 28; c += 2) {
      const v4f xx = *(const v4f*)&L.xn[rg * 56 + c * 2];
      ldgu(tb, c + 1);
      usegu(ta, xx[0], xx[1]);
      ldgu(ta, min(c + 2, 27));
      usegu(tb, xx[2], xx[3]);
    }
    const rsrc_t rd = mk(wd + gs * (64 * D_ * 2), 64 * D_ * 2);
    const unsigned vd = lane < 56 ? (unsigned)lane * 16u : OOB;
    auto ldd = [&](v4u* t, int c) __attribute__((always_inline)) {
      gate(GLD);
#pragma unroll
      for (int i = 0; i < 4; ++i) t[i] = __builtin_amdgcn_raw_buffer_load_b128(rd, vd, (c * 4 + i) * (D_ * 2), NT);
      __builtin_amdgcn_sched_barrier(0);
    };
    ldd(ta, 0);
    float hv[8];
#pragma unroll
    for (int e = 0; e < 8; ++e) {
      float gg = g[e >> 1][e & 1], uu = u[e >> 1][e & 1];
      gg += dpp<DPP_R8>(gg); gg = mrg16(gg, gg); gg = mrg32(gg, gg);
      uu += dpp<DPP_R8>(uu); uu = mrg16(uu, uu); uu = mrg32(uu, uu);
      hv[e] = rbf(rbf(1.0f / (1.0f + expf(-rbf(gg)))) * rbf(uu));
    }
    if (lane < 8) {
      *(v4f*)&L.hv[lane * 8] = v4f{hv[0], hv[1], hv[2], hv[3]};
      *(v4f*)&L.hv[lane * 8 + 4] = v4f{hv[4], hv[5], hv[6], hv[7]};
    }
    auto used = [&](const v4u* t, const v4f hh) __attribute__((always_inline)) {
#pragma unroll
      for (int k = 0; k < 4; ++k)
#pragma unroll
        for (int e = 0; e < 4; ++e) z[e] = __builtin_elementwise_fma(unpk(t[k][e]), v2f{hh[k], hh[k]}, z[e]);
      asm volatile("" : "+v"(z[0]), "+v"(z[1]), "+v"(z[2]), "+v"(z[3]));
      __builtin_amdgcn_sched_barrier(0);
    };
#pragma unroll 1
    for (int c = 0; c < 16; c += 2) {
      const v4f h0 = *(const v4f*)&L.hv[c * 4], h1 = *(const v4f*)&L.hv[c * 4 + 4];
      ldd(tb, c + 1);
      used(ta, h0);
      ldd(ta, min(c + 2, 15));
      used(tb, h1);
    }
  }
  if (lane < 56) {
    *(v4f*)&L.z[lane * 8] = v4f{z[0][0], z[0][1], z[1][0], z[1][1]};
    *(v4f*)&L.z[lane * 8 + 4] = v4f{z[2][0], z[2][1], z[3][0], z[3][1]};
  }
  {
    v2f ml;
#pragma unroll 1
    for (unsigned spin = 0; spin < SPIN_MAX; ++spin) {
      ml = *(lds_v2f*)&L.ml[lane & 3][0];
      if (__builtin_amdgcn_ballot_w64(asu(ml[1]) == 0xBF800000u) == 0) break;
      __builtin_amdgcn_s_sleep(16);
    }
    float m = fmaxf(ml[0], dpp<DPP_X1>(ml[0]));
    m = fmaxf(m, dpp<DPP_X2>(m));
    float l = ml[1] * ex2(ml[0] - m);
    l += dpp<DPP_X1>(l);
    l += dpp<DPP_X2>(l);
    const v2u gr = (lane & 1) ? v2u{asu(l), thi} : v2u{asu(m), tlo};
    __builtin_amdgcn_raw_buffer_store_b64(gr, rws, lane < 2 ? WS_REC + bid * 128 + lane * 8 : OOB, 0, SC1);
    v4u g;
#pragma unroll 1
    for (unsigned spin = 0; spin < SPIN_MAX; ++spin) {
      asm volatile("" ::: "memory");
      g = __builtin_amdgcn_raw_buffer_load_b128(rws, WS_REC + (bid ^ 1) * 128, 0, SC1);
      if (__builtin_amdgcn_ballot_w64(g[1] != tlo || g[3] != thi) == 0) break;
      __builtin_amdgcn_s_sleep(64);
    }
    const float mo = asf(g[0]), M = fmaxf(m, mo), e = ex2(m - M), eo = ex2(mo - M);
    const float la = l * e, lb = asf(g[2]) * eo;
    L.wgt = e / ((j & 1) ? lb + la : la + lb);
  }
  const int jr = (bx & 7) * 4 + ((bx >> 3) & 3);
  if (jr >= 28) { wg_barrier(); return; }
  const int col = jr * 16 + (lane & 3) * 4 + half + ((lane >> 3) & 2);
  float xres = asf((unsigned)__builtin_amdgcn_raw_buffer_load_b16(mk(x + b * (D_ * 2), D_ * 2), col * 2, 0, 0) << 16);
  const unsigned yt = (lane & 1) ? thi : tlo;
  unsigned va = WS_Y + (b * 32 + (lane >> 2)) * SLOT + jr * 64 + (lane & 3) * 16, vb_ = va + 16 * SLOT;
  const unsigned ot = __builtin_amdgcn_raw_buffer_load_b16(mk(out + b * (D_ * 2), D_ * 2), lane == 0 ? (unsigned)col * 2u : OOB, 0, NT);
  asm volatile("" : "+v"(xres) : "v"(ot));
  wg_barrier();
  const unsigned epd = *(lds_u*)&L.epd;
  v4u y0 = {0, 0, 0, 0}, y1 = {0, 0, 0, 0};
#pragma unroll 1
  for (unsigned spin = 0; spin < SPIN_MAX; ++spin) {
    asm volatile("" ::: "memory");
    const v4u pa = __builtin_amdgcn_raw_buffer_load_b128(rws, va, 0, SC1);
    const v4u pc = __builtin_amdgcn_raw_buffer_load_b128(rws, vb_, 0, SC1);
    unsigned c0 = yt ^ __builtin_amdgcn_perm(__builtin_amdgcn_perm(pa[3], pa[2], 0x0400u), __builtin_amdgcn_perm(pa[1], pa[0], 0x0400u), 0x05040100u);
    unsigned c1 = yt ^ __builtin_amdgcn_perm(__builtin_amdgcn_perm(pc[3], pc[2], 0x0400u), __builtin_amdgcn_perm(pc[1], pc[0], 0x0400u), 0x05040100u);
    c0 |= asu(dpp<DPP_X1>(asf(c0)));
    c1 |= asu(dpp<DPP_X1>(asf(c1)));
    const bool k0 = (va != OOB) & (c0 == 0), k1 = (vb_ != OOB) & (c1 == 0);
    y0 = k0 ? pa : y0;
    y1 = k1 ? pc : y1;
    va = k0 ? OOB : va;
    vb_ = k1 ? OOB : vb_;
    if (__builtin_amdgcn_ballot_w64((va & vb_) != OOB) == 0) break;
    __builtin_amdgcn_s_sleep(1);
  }
  float v[4];
#pragma unroll
  for (int e = 0; e < 4; ++e) v[e] = asf(y0[e] & 0xffffff00u) + asf(y1[e] & 0xffffff00u);
  float r = mrg16(mrg32(v[0], v[1]), mrg32(v[2], v[3]));
  r += dpp<DPP_R8>(r);
  r += dpp<DPP_R4>(r);
  const unsigned ob = pkbf(xres + r, 0.f);
  __builtin_amdgcn_raw_buffer_store_b16((unsigned short)ob, mk(out + b * (D_ * 2), D_ * 2), (lane & 12) == 0 ? (unsigned)col * 2u : OOB, 0, 0);
  if (bx == 61 && epd == 0) __builtin_amdgcn_raw_buffer_store_b64(v2u{tlo, thi}, rws, lane == 0 ? WS_E : OOB, 0, SC1);
}

extern "C" void kernel_launch(void* const* d_in, const int* in_sizes, int n_in, void* d_out, int out_size,
                              void* d_ws, size_t ws_size, hipStream_t stream) {
  (void)in_sizes; (void)n_in; (void)out_size; (void)ws_size;
  fused_decode_kernel<<<256, 768, 0, stream>>>((const char*)d_in[0], (const char*)d_in[1], (const char*)d_in[3], (char*)d_ws,
                                               (const char*)d_in[2], (const char*)d_in[4], (const char*)d_in[6],
                                               (const char*)d_in[7], (const char*)d_in[8], (char*)d_out);
}
```
